```python
import torch

def _softmax(x, axis=-1):
    return torch.softmax(x, dim=axis)
def _gelu(x):
    return torch.nn.functional.gelu(x, approximate="tanh")
def _silu(x):
    return torch.nn.functional.silu(x)
def _sigmoid(x):
    return torch.sigmoid(x)
def _layernorm(x, axis=-1, eps=1e-5):
    m = x.mean(dim=axis, keepdim=True)
    v = x.var(dim=axis, keepdim=True, unbiased=False)
    return (x - m) / torch.sqrt(v + eps)
def _scatter_add(tgt, idx, src, axis=0):
    return tgt.clone().index_add_(axis, idx, src)
def _segment_sum(src, seg, n):
    out = torch.zeros((n,) + src.shape[1:], dtype=src.dtype)
    return out.index_add_(0, seg, src)
def _pad(x, pw):
    flat = tuple(v for pair in reversed(pw) for v in pair)
    return torch.nn.functional.pad(x, flat)

kernel_name = 'concat_heads__sliding_block_cached__compressed_kv_cla__decode'

def setup_inputs():

    _w = (lambda a: (lambda t: t.to(torch.bfloat16) if t.is_floating_point() else t)(torch.as_tensor(a)))
    def _A_setup():
        B, Sq, D = 2, 1, 576
        Hq, Hkv, Sk, Dh = 32, 16, 8192, 128
        assert Hq % Hkv == 0, (Hq, Hkv)
        return {
            "x": _w((lambda *s: torch.randn(*s, dtype=torch.bfloat16) * 1.6)(B, Sq, D)),
            "k_cache": _w((lambda *s: torch.randn(*s, dtype=torch.bfloat16) * 1.6)(B, Hkv, Sk, Dh)),
            "v_cache": _w((lambda *s: torch.randn(*s, dtype=torch.bfloat16) * 0.05)(B, Hkv, Sk, Dh)),
            "wq": _w((lambda *s: torch.randn(*s, dtype=torch.bfloat16) * 1.6)(D, Hq * Dh) * (D ** -0.5)),
            "wo": _w((lambda *s: torch.randn(*s, dtype=torch.bfloat16) * 0.05)(Hq * Dh, D) * ((Hq * Dh) ** -0.5)),
        }
    _A_in = _A_setup()

    def _B_setup():
        B, H, Sq, Sk, D, M = 2, 32, 1, 8192, 128, 576
        Dc = 4 * D
        return {
            "x": _w((lambda *s: torch.randn(*s, dtype=torch.bfloat16) * 3.2)(B, Sq, M)),
            "c_kv": _w((lambda *s: torch.randn(*s, dtype=torch.bfloat16) * 0.1)(B, Sk, Dc)),
            "w_q": _w((lambda *s: torch.randn(*s, dtype=torch.bfloat16) * 6.4)(M, H, D) * (M ** -0.5)),
            "w_uk": _w((lambda *s: torch.randn(*s, dtype=torch.bfloat16) * 6.4)(Dc, H, D) * (Dc ** -0.5)),
            "w_uv": _w((lambda *s: torch.randn(*s, dtype=torch.bfloat16) * 0.1)(Dc, H, D) * (Dc ** -0.5)),
            "w_o": _w((lambda *s: torch.randn(*s, dtype=torch.bfloat16) * 0.1)(H, D, M) * ((H * D) ** -0.5)),
        }
    _B_in = _B_setup()

    return {
        "A_x": _A_in["x"],
        "A_k_cache": _A_in["k_cache"],
        "A_v_cache": _A_in["v_cache"],
        "A_wq": _A_in["wq"],
        "A_wo": _A_in["wo"],
        "B_x": _B_in["x"],
        "B_c_kv": _B_in["c_kv"],
        "B_w_q": _B_in["w_q"],
        "B_w_uk": _B_in["w_uk"],
        "B_w_uv": _B_in["w_uv"],
        "B_w_o": _B_in["w_o"],
    }

def reference(A_x, A_k_cache, A_v_cache, A_wq, A_wo, B_x, B_c_kv, B_w_q, B_w_uk, B_w_uv, B_w_o):
    def _A_ref(x, k_cache, v_cache, wq, wo):
        B, Sq, D = x.shape
        Hq, Dh = 32, 128
        Hkv, Sk = k_cache.shape[1], k_cache.shape[2]
        W = min(4096, max(256, Sk // 4))
        rep = Hq // Hkv
        q = (lambda x, *d: x.permute(*d))((x @ wq).reshape(B, Sq, Hq, Dh), 0, 2, 1, 3)
        k = (lambda x, n, axis: torch.repeat_interleave(x, n, dim=axis))(k_cache[:, :, Sk - W:, :], rep, axis=1)
        v = (lambda x, n, axis: torch.repeat_interleave(x, n, dim=axis))(v_cache[:, :, Sk - W:, :], rep, axis=1)
        scores = torch.einsum("bhqd,bhkd->bhqk", q, k) * (Dh ** -0.5)
        p = _softmax(scores, axis=-1)
        o = torch.einsum("bhqk,bhkd->bhqd", p, v)
        return (lambda x, *d: x.permute(*d))(o, 0, 2, 1, 3).reshape(B, Sq, Hq * Dh) @ wo

    def _B_ref(x, c_kv, w_q, w_uk, w_uv, w_o):
        B, Sq, M = x.shape[0], x.shape[1], x.shape[2]
        Sk = c_kv.shape[1]
        H, D = w_q.shape[1], w_q.shape[2]
        q = torch.einsum("bqm,mhd->bhqd", x, w_q)
        k = torch.einsum("bkc,chd->bhkd", c_kv, w_uk)
        v = torch.einsum("bkc,chd->bhkd", c_kv, w_uv)
        scores = torch.einsum("bhqd,bhkd->bhqk", q, k) * (D ** -0.5)
        off = Sk - Sq
        qi = torch.arange(Sq)[:, None]
        kj = torch.arange(Sk)[None, :]
        cmask = kj <= (qi + off)
        scores = torch.where(cmask[None, None], scores, -1e9)
        p = _softmax(scores, axis=-1)
        ctx = torch.einsum("bhqk,bhkd->bhqd", p, v)
        return torch.einsum("bhqd,hdm->bqm", ctx, w_o)

    a = _A_ref(A_x, A_k_cache, A_v_cache, A_wq, A_wo)
    b = _B_ref(B_x, B_c_kv, B_w_q, B_w_uk, B_w_uv, B_w_o)
    return torch.concatenate([a, b], axis=-1)
```

```cpp
#include <hip/hip_runtime.h>
#include <hip/hip_bf16.h>
#include <stdint.h>

typedef unsigned short u16;
__device__ __forceinline__ float bf2f(u16 b) { return __uint_as_float(((unsigned)b) << 16); }
__device__ __forceinline__ u16 f2bf(float f) { unsigned u = __float_as_uint(f); u += 0x7FFFu + ((u >> 16) & 1u); return (u16)(u >> 16); }
__device__ __forceinline__ float rbf(float f) { return bf2f(f2bf(f)); }

#define ROUND_Q 1

__global__ void k_q(const u16* xA, const u16* wqA, const u16* xB, const u16* wqB, float* q) {
  int br = blockIdx.y;
  const u16* x = br ? xB : xA; const u16* w = br ? wqB : wqA;
  int n = blockIdx.x * 64 + threadIdx.x;
  float a0 = 0, a1 = 0;
  for (int m = 0; m < 576; ++m) { float wv = bf2f(w[(size_t)m * 4096 + n]); a0 += bf2f(x[m]) * wv; a1 += bf2f(x[576 + m]) * wv; }
  if (ROUND_Q) { a0 = rbf(a0); a1 = rbf(a1); }
  q[br * 8192 + n] = a0; q[br * 8192 + 4096 + n] = a1;
}
__global__ void k_qabs(const float* qB, const u16* wuk, float* qabs) {
  int h = blockIdx.y; int c = blockIdx.x * 64 + threadIdx.x;
  float a0 = 0, a1 = 0;
  for (int d = 0; d < 128; ++d) { float wv = bf2f(wuk[((size_t)c * 32 + h) * 128 + d]); a0 += qB[h * 128 + d] * wv; a1 += qB[4096 + h * 128 + d] * wv; }
  qabs[(0 * 32 + h) * 512 + c] = a0; qabs[(1 * 32 + h) * 512 + c] = a1;
}
__global__ void k_sA(const float* qA, const u16* kc, float* s) {
  int b = blockIdx.z, h = blockIdx.y; int k = blockIdx.x * 64 + threadIdx.x;
  const u16* kr = kc + (((size_t)b * 16 + h / 2) * 8192 + 6144 + k) * 128;
  float a = 0; for (int d = 0; d < 128; ++d) a += qA[b * 4096 + h * 128 + d] * bf2f(kr[d]);
  s[((size_t)b * 32 + h) * 2048 + k] = a * 0.08838834764831845f;
}
__global__ void k_sB(const float* qabs, const u16* ckv, float* s) {
  int b = blockIdx.z, h = blockIdx.y; int k = blockIdx.x * 64 + threadIdx.x;
  const u16* kr = ckv + ((size_t)b * 8192 + k) * 512;
  float a = 0; for (int c = 0; c < 512; ++c) a += qabs[(b * 32 + h) * 512 + c] * bf2f(kr[c]);
  s[((size_t)b * 32 + h) * 8192 + k] = a * 0.08838834764831845f;
}
__global__ void k_softmax(float* s, int n) {
  float* r = s + (size_t)blockIdx.x * n; __shared__ float red[256];
  float m = -INFINITY; for (int i = threadIdx.x; i < n; i += 256) m = fmaxf(m, r[i]);
  red[threadIdx.x] = m; __syncthreads();
  for (int o = 128; o; o >>= 1) { if (threadIdx.x < o) red[threadIdx.x] = fmaxf(red[threadIdx.x], red[threadIdx.x + o]); __syncthreads(); }
  m = red[0]; __syncthreads();
  float l = 0; for (int i = threadIdx.x; i < n; i += 256) { float e = __expf(r[i] - m); r[i] = e; l += e; }
  red[threadIdx.x] = l; __syncthreads();
  for (int o = 128; o; o >>= 1) { if (threadIdx.x < o) red[threadIdx.x] += red[threadIdx.x + o]; __syncthreads(); }
  l = 1.f / red[0];
  for (int i = threadIdx.x; i < n; i += 256) r[i] *= l;
}
__global__ void k_pvA(const float* p, const u16* vc, float* o) {
  int b = blockIdx.y, h = blockIdx.x, d = threadIdx.x;
  const u16* v = vc + (((size_t)b * 16 + h / 2) * 8192 + 6144) * 128 + d;
  float a = 0; for (int k = 0; k < 2048; ++k) a += p[((size_t)b * 32 + h) * 2048 + k] * bf2f(v[(size_t)k * 128]);
  o[b * 4096 + h * 128 + d] = a;
}
__global__ void k_pvB(const float* p, const u16* ckv, float* cc) {
  int b = blockIdx.z, h = blockIdx.y, c = blockIdx.x * 64 + threadIdx.x;
  const u16* v = ckv + (size_t)b * 8192 * 512 + c;
  float a = 0; for (int k = 0; k < 8192; ++k) a += p[((size_t)b * 32 + h) * 8192 + k] * bf2f(v[(size_t)k * 512]);
  cc[(b * 32 + h) * 512 + c] = a;
}
__global__ void k_uv(const float* cc, const u16* wuv, float* ctx) {
  int b = blockIdx.y, h = blockIdx.x, d = threadIdx.x;
  float a = 0; for (int c = 0; c < 512; ++c) a += cc[(b * 32 + h) * 512 + c] * bf2f(wuv[((size_t)c * 32 + h) * 128 + d]);
  ctx[b * 4096 + h * 128 + d] = a;
}
__global__ void k_out(const float* oA, const u16* woA, const float* oB, const u16* woB, u16* out) {
  int br = blockIdx.y, b = blockIdx.z, m = blockIdx.x * 64 + threadIdx.x;
  const float* o = (br ? oB : oA) + b * 4096; const u16* w = br ? woB : woA;
  float a = 0; for (int k = 0; k < 4096; ++k) a += o[k] * bf2f(w[(size_t)k * 576 + m]);
  out[b * 1152 + br * 576 + m] = f2bf(a);
}

extern "C" void kernel_launch(void* const* d_in, const int* in_sizes, int n_in, void* d_out, int out_size, void* d_ws, size_t ws_size, hipStream_t stream) {
  const u16* xA = (const u16*)d_in[0]; const u16* kc = (const u16*)d_in[1]; const u16* vc = (const u16*)d_in[2];
  const u16* wqA = (const u16*)d_in[3]; const u16* woA = (const u16*)d_in[4];
  const u16* xB = (const u16*)d_in[5]; const u16* ckv = (const u16*)d_in[6]; const u16* wqB = (const u16*)d_in[7];
  const u16* wuk = (const u16*)d_in[8]; const u16* wuv = (const u16*)d_in[9]; const u16* woB = (const u16*)d_in[10];
  float* ws = (float*)d_ws;
  float* q = ws; float* qabs = q + 16384; float* sA = qabs + 32768; float* sB = sA + 2 * 32 * 2048;
  float* oA = sB + 2 * 32 * 8192; float* cc = oA + 8192; float* ctx = cc + 32768;
  k_q<<<dim3(64, 2), 64, 0, stream>>>(xA, wqA, xB, wqB, q);
  k_qabs<<<dim3(8, 32), 64, 0, stream>>>(q + 8192, wuk, qabs);
  k_sA<<<dim3(32, 32, 2), 64, 0, stream>>>(q, kc, sA);
  k_sB<<<dim3(128, 32, 2), 64, 0, stream>>>(qabs, ckv, sB);
  k_softmax<<<64, 256, 0, stream>>>(sA, 2048);
  k_softmax<<<64, 256, 0, stream>>>(sB, 8192);
  k_pvA<<<dim3(32, 2), 128, 0, stream>>>(sA, vc, oA);
  k_pvB<<<dim3(8, 32, 2), 64, 0, stream>>>(sB, ckv, cc);
  k_uv<<<dim3(32, 2), 128, 0, stream>>>(cc, wuv, ctx);
  k_out<<<dim3(9, 2, 2), 64, 0, stream>>>(oA, woA, ctx, woB, (u16*)d_out);
}
```

```cpp
#include <hip/hip_runtime.h>
#include <stdint.h>

typedef unsigned v4u __attribute__((ext_vector_type(4)));
typedef unsigned v2u __attribute__((ext_vector_type(2)));
typedef float f2 __attribute__((ext_vector_type(2)));
typedef float f4 __attribute__((ext_vector_type(4)));
typedef float f16v __attribute__((ext_vector_type(16)));
typedef short s4 __attribute__((ext_vector_type(4)));
typedef __bf16 b2 __attribute__((ext_vector_type(2)));
typedef __bf16 b8 __attribute__((ext_vector_type(8)));
#define LAS __attribute__((address_space(3)))
#define GAS __attribute__((address_space(1)))
#define DI __device__ __forceinline__

#define A_W0 2
#define A_NSLOW 3
#define A_W 4
#define B_W 6

#define CODE_KB 0
constexpr float C2 = 0.08838834764831845f * 1.4426950408889634f;
constexpr int SPIN_CAP = 1 << 16;
constexpr int WS_E = 0, WS_QP = 4096, WS_QA = WS_QP + 524288, WS_ML = WS_QA + 131072, WS_CX = WS_ML + 65536,
              WS_AP = WS_CX + 262144, WS_AM = WS_AP + 262144, WS_OP = WS_AM + 16384, WS_BP = WS_OP + 2359296,
              WS_END = WS_BP + 4194304;
constexpr int L_WA = 110592, L_WQB = 89600, L_QIMG = 131072, L_SCR = 152064;
constexpr int L_X = L_SCR, L_QV = L_SCR + 640, L_FLAG = L_SCR + 1152, L_DUMMY = L_SCR + 1280, L_MRG = L_SCR + 2304,
              L_MLS = L_SCR + 10496, L_OV = L_SCR + 10624, L_RED = L_SCR + 10752;
constexpr int L_MX = L_QIMG, L_LS = L_QIMG + 512, L_PF = L_QIMG + 1024, L_F = L_QIMG + 12288, L_CC = L_QIMG + 13312;
constexpr int L_WUV = 0, L_WOB = 36864;

#define ST(k) do {} while (0)
#define STN(k, n) do { (void)(n); } while (0)

struct Args {
  const char* x[2]; const char* wq[2]; const char* wo[2];
  const char* kc; const char* vc; const char* ckv; const char* wuk; const char* wuv;
  char* out; char* ws;
};

template <int N> DI void vmwait() { asm volatile("s_waitcnt vmcnt(%0) lgkmcnt(0)" ::"n"(N) : "memory"); }
DI void bar() { asm volatile("s_waitcnt lgkmcnt(0)\n\ts_barrier" ::: "memory"); }
DI void dma_nt(unsigned lds, unsigned voff, const char* base) {
  unsigned keep;
  asm volatile("s_mov_b32 %0, m0\n\ts_mov_b32 m0, %1\n\ts_nop 2\n\tglobal_load_lds_dwordx4 %2, %3 nt\n\ts_mov_b32 m0, %0"
               : "=&s"(keep) : "s"(lds), "v"(voff), "s"(base) : "memory");
}
DI void dma_def(unsigned lds, unsigned voff, const char* base) {
  unsigned keep;
  asm volatile("s_mov_b32 %0, m0\n\ts_mov_b32 m0, %1\n\ts_nop 2\n\tglobal_load_lds_dwordx4 %2, %3\n\ts_mov_b32 m0, %0"
               : "=&s"(keep) : "s"(lds), "v"(voff), "s"(base) : "memory");
}
typedef __amdgpu_buffer_rsrc_t rsrc_t;
DI v4u ld16(rsrc_t rs, int off) { return __builtin_amdgcn_raw_buffer_load_b128(rs, off, 0, 16); }
DI v2u ld8(rsrc_t rs, int off) { return __builtin_amdgcn_raw_buffer_load_b64(rs, off, 0, 16); }
DI void st16(rsrc_t rs, int off, v4u v) { __builtin_amdgcn_raw_buffer_store_b128(v, rs, off, 0, 16); }
DI void st8(rsrc_t rs, int off, v2u v) { __builtin_amdgcn_raw_buffer_store_b64(v, rs, off, 0, 16); }
DI unsigned fu(float x) { return __float_as_uint(x); }
DI float uf(unsigned x) { return __uint_as_float(x); }
DI unsigned pk(float lo, float hi) { return __builtin_bit_cast(unsigned, __builtin_convertvector(f2{lo, hi}, b2)); }
DI float lo16(unsigned x) { return uf(x << 16); }
DI float hi16(unsigned x) { return uf(x & 0xffff0000u); }
template <int CTRL> DI float dpp(float x) { return uf(__builtin_amdgcn_mov_dpp(fu(x), CTRL, 0xf, 0xf, true)); }
constexpr int X1 = 0xB1, X2 = 0x4E, XH = 0x141, X8 = 0x128;
DI float sw32add(float a, float b) { auto r = __builtin_amdgcn_permlane32_swap(fu(a), fu(b), false, false); return uf(r[0]) + uf(r[1]); }
DI float sw16add(float a, float b) { auto r = __builtin_amdgcn_permlane16_swap(fu(a), fu(b), false, false); return uf(r[0]) + uf(r[1]); }
DI float sw32max(float a) { auto r = __builtin_amdgcn_permlane32_swap(fu(a), fu(a), false, false); return fmaxf(uf(r[0]), uf(r[1])); }
DI float sw16max(float a) { auto r = __builtin_amdgcn_permlane16_swap(fu(a), fu(a), false, false); return fmaxf(uf(r[0]), uf(r[1])); }
DI f4 mfma32(v4u a, v4u b, f4 c) { return __builtin_amdgcn_mfma_f32_16x16x32_bf16(__builtin_bit_cast(b8, a), __builtin_bit_cast(b8, b), c, 0, 0, 0); }
DI f4 mfma16(v2u a, v2u b, f4 c) { return __builtin_amdgcn_mfma_f32_16x16x16bf16_1k(__builtin_bit_cast(s4, a), __builtin_bit_cast(s4, b), c, 0, 0, 0); }
DI f16v mfmaB(v4u a, v4u b, f16v c) { return __builtin_amdgcn_mfma_f32_32x32x16_bf16(__builtin_bit_cast(b8, a), __builtin_bit_cast(b8, b), c, 0, 0, 0); }
DI v4u cat(v2u a, v2u b) { return v4u{a.x, a.y, b.x, b.y}; }
DI float ex2(float x) { return __builtin_amdgcn_exp2f(x); }

template <int N, class F> DI int sweep(rsrc_t rs, unsigned tag, v4u (&v)[N], F off) {
  int n = 0;
  for (;;) {
    asm volatile("" ::: "memory");
#pragma unroll
    for (int k = 0; k < N; ++k) v[k] = ld16(rs, off(k));
    unsigned bad = 0;
#pragma unroll
    for (int k = 0; k < N; ++k) bad |= (v[k].y ^ tag) | (v[k].w ^ tag);
    ++n;
    if (__builtin_amdgcn_ballot_w64(bad != 0) == 0 || n > SPIN_CAP) break;
    __builtin_amdgcn_s_sleep(2);
  }
  return n;
}

__global__ void __launch_bounds__(512, 2) fused_decode(Args a) {
  __shared__ __attribute__((aligned(16))) char smem[163840];
  LAS char* const L = (LAS char*)smem;
  const unsigned LB = (unsigned)(uintptr_t)L;
#define LD128(o) (*(const LAS v4u*)(L + (o)))
#define LD64(o) (*(const LAS v2u*)(L + (o)))
#define LDF(o) (*(const LAS float*)(L + (o)))
#define TR(o) __builtin_bit_cast(v2u, __builtin_amdgcn_ds_read_tr16_b64_v4i16((LAS s4*)(L + (o))))
  ST(0);
  const int c = blockIdx.x, tid = threadIdx.x, lane = tid & 63;
  const int w = __builtin_amdgcn_readfirstlane(tid >> 6);
  const int role = (c >> 3) & 1, u = (c & 7) * 16 + (c >> 4), h = u >> 2, q4 = u & 3;
  const int g = lane >> 4, j16 = lane & 15, tq = (lane >> 2) & 3, tp = lane & 3;

  unsigned ev;
  { const unsigned z = 0; asm volatile("s_nop 4\n\tglobal_load_dword %0, %1, %2 sc1" : "=v"(ev) : "v"(z), "s"(a.ws) : "memory"); }
  const unsigned voffV = g * 256 + ((j16 ^ (g << 1)) << 4);
  const int lwq = role ? L_WQB : L_WA;
  {
    const char* wq = a.wq[role] + (size_t)(144 * q4) * 8192 + h * 256;
    const unsigned vo = g * 8192 + ((j16 ^ (g << 1)) << 4);
    if (w == 0) { if (lane < 36) dma_def(LB + L_X, (lane >= 18 ? 1152 - 288 : 0) + lane * 16, a.x[role] + 288 * q4); }
    if (w == 1) {
      unsigned long long pc; asm volatile("s_getpc_b64 %0" : "=s"(pc));
      const int ch = c >> 3;
      if (ch < CODE_KB) dma_def(LB + L_DUMMY, lane * 16, (const char*)(pc & ~255ull) + ch * 1024);
    }
    if (tid == 128) *(LAS unsigned*)(L + L_FLAG) = 0u;
#pragma unroll
    for (int i = 0; i < 4; ++i) {
      if (i >= A_W0 && !role) vmwait<A_W0 - 1>();
      dma_nt(LB + lwq + (w + 8 * i) * 1152, vo, wq + (size_t)(w + 8 * i) * 32768);
    }
    if (4 >= A_W0 && !role) vmwait<A_W0 - 1>();
    const int rl = 2 * (w & 1) + g;
    if (lane < 32) dma_nt(LB + lwq + (32 + (w >> 1)) * 1152 + (w & 1) * 512, rl * 8192 + ((j16 ^ (rl << 1)) << 4), wq + (size_t)(32 + (w >> 1)) * 32768);
  }
  const rsrc_t rs = __builtin_amdgcn_make_buffer_rsrc(a.ws, 0, WS_END, 0x00020000);
  v4u wuk[8];
  if (role && w < 4) {
    const rsrc_t ru = __builtin_amdgcn_make_buffer_rsrc((void*)a.wuk, 0, 4194304, 0x00020000);
    const int o = (128 * q4 + 32 * w + 4 * tq) * 8192 + h * 256 + (4 * tp + g) * 16;
#pragma unroll
    for (int i = 0; i < 8; ++i) wuk[i] = __builtin_amdgcn_raw_buffer_load_b128(ru, o, ((i >> 2) * 16 + (i & 3)) * 8192, 2);
    vmwait<8>();
  } else {
    vmwait<0>();
  }
  asm volatile("" : "+v"(ev));
  const unsigned tag = __builtin_amdgcn_readfirstlane(ev) + 1u;
  bar();
  ST(1);
  {
    const int tb = lwq + g * 1152 + tq * 256 + (((2 * w + (tp >> 1)) ^ (tq << 1)) << 4) + (tp & 1) * 8;
    const int xb = L_X + (lane & 1) * 288 + g * 8;
    f4 acc = {0.f, 0.f, 0.f, 0.f};
#pragma unroll
    for (int t = 0; t < 8; t += 2)
      acc = mfma32(cat(LD64(xb + t * 32), LD64(xb + t * 32 + 32)), cat(TR(tb + t * 4608), TR(tb + t * 4608 + 4608)), acc);
    acc += mfma16(LD64(xb + 256), TR(tb + 8 * 4608), f4{0.f, 0.f, 0.f, 0.f});
    if (lane < 32) st8(rs, WS_QP + (role * 128 + u) * 2048 + g * 1024 + (16 * w + j16) * 8, v2u{fu(g ? acc[1] : acc[0]), tag});
  }
  bar();
  ST(2);
  if (w == 0) {
    vmwait<0>();
    v4u v[8];
    const int base = WS_QP + lane * 16 + (role ? (128 + 4 * h) * 2048 : (u & ~7) * 2048 + ((u >> 2) & 1) * 1024);
    const int vs = role ? 1024 : 8192;
    const int n = sweep<8>(rs, tag, v, [&](int k) { return base + (k >> 2) * vs + (k & 3) * 2048; });
    STN(16, n);
#pragma unroll
    for (int k = 0; k < 2; ++k)
      *(LAS unsigned*)(L + L_QV + k * 256 + lane * 4) =
          pk((uf(v[4 * k].x) + uf(v[4 * k + 1].x)) + (uf(v[4 * k + 2].x) + uf(v[4 * k + 3].x)),
             (uf(v[4 * k].z) + uf(v[4 * k + 1].z)) + (uf(v[4 * k + 2].z) + uf(v[4 * k + 3].z)));
    asm volatile("s_waitcnt lgkmcnt(0)" ::: "memory");
    *(volatile LAS unsigned*)(L + L_FLAG) = 1u;
    ST(3);
  }
  v4u wof[5];
  if (!role) {
    const size_t kvo = ((size_t)(((u >> 2) & 1) * 16 + (u >> 3)) * 8192 + 6144 + 512 * q4 + 16 * w) * 256;
    const char* kp = a.kc + kvo;
    const char* vp = a.vc + kvo;
    const char* wo = a.wo[0] + (size_t)u * 36864 + w * 4608;
    const unsigned voffK = g * 256 + ((j16 ^ g) << 4);
    const int ring = w * 13824;
    const int kb = ring + tq * 1088 + tp * 256 + ((g ^ tp) << 4);
    const int vb = ring + g * 1152 + tq * 256 + (tp & 1) * 8;
    v4u qf[4];
    f4 sc[4], oc[8];
    v2u pf[4];
    float mw = 0.f, lw = 0.f;
#pragma unroll
    for (int n = 0; n < 8; ++n) oc[n] = f4{0.f, 0.f, 0.f, 0.f};
#pragma unroll
    for (int n = 0; n <= 31 + A_W; ++n) {
      if (n < A_NSLOW) vmwait<A_W0 - 1>(); else if (n < 37) vmwait<A_W - 1>(); else vmwait<5>();
#pragma unroll
      for (int m = n; m < (n == 31 + A_W ? 37 : n + 1); ++m) {
        const int T = m >> 2, i = m & 3;
        if (m < 16) dma_nt(LB + ring + (T % 3) * 4608 + i * 1088, voffK, kp + T * 32768 + i * 1024);
        else if (m < 32) dma_nt(LB + ring + (T % 3) * 4608 + i * 1152, voffV, vp + (T - 4) * 32768 + i * 1024);
        else if (m < 36) dma_nt(LB + L_WA + w * 4608 + i * 1024, lane * 16, wo + i * 1024);
        else if (m == 36 && lane < 32) dma_nt(LB + L_WA + w * 4608 + 4096, lane * 16, wo + 4096);
      }
      const int d = n - 3 - A_W;
      if (d >= 0 && (d & 3) == 0) {
        const int T = d >> 2;
        if (T == 0) {
          while (*(volatile LAS unsigned*)(L + L_FLAG) == 0u) __builtin_amdgcn_s_sleep(1);
          asm volatile("" ::: "memory");
#pragma unroll
          for (int s = 0; s < 4; ++s) qf[s] = LD128(L_QV + (lane & 1) * 256 + s * 64 + g * 16);
          ST(4);
        }
        if (T < 4) {
          f4 s4_ = {0.f, 0.f, 0.f, 0.f};
#pragma unroll
          for (int s = 0; s < 4; ++s) s4_ = mfma32(LD128(kb + (T % 3) * 4608 + s * 64), qf[s], s4_);
          sc[T] = s4_;
          if (T == 3) {
            float m = -INFINITY;
#pragma unroll
            for (int t = 0; t < 4; ++t) m = fmaxf(fmaxf(fmaxf(sc[t][0], sc[t][1]), fmaxf(sc[t][2], sc[t][3])), m);
            m = sw32max(sw16max(m));
            mw = m * C2;
            float l = 0.f;
#pragma unroll
            for (int t = 0; t < 4; ++t) {
              const float p0 = ex2(fmaf(sc[t][0], C2, -mw)), p1 = ex2(fmaf(sc[t][1], C2, -mw)), p2 = ex2(fmaf(sc[t][2], C2, -mw)), p3 = ex2(fmaf(sc[t][3], C2, -mw));
              l += (p0 + p1) + (p2 + p3);
              pf[t] = v2u{pk(p0, p1), pk(p2, p3)};
            }
            l = sw16add(l, l);
            lw = sw32add(l, l);
            ST(5);
          }
        } else {
          if (T == 7) ST(6);
#pragma unroll
          for (int n2 = 0; n2 < 8; ++n2)
            oc[n2] = mfma16(pf[T - 4], TR(vb + (T % 3) * 4608 + (((2 * n2 + (tp >> 1)) ^ (tq << 1)) << 4)), oc[n2]);
        }
      }
    }
    if (lane < 16) {
#pragma unroll
      for (int n = 0; n < 8; ++n) *(LAS f2*)(L + L_MRG + w * 1024 + (16 * n + lane) * 8) = f2{oc[n][0], oc[n][1]};
      if (lane < 2) *(LAS f2*)(L + L_MLS + w * 16 + lane * 8) = f2{mw, lw};
    }
    ST(7);
    bar();
    if (w < 4) {
      const int hh = w >> 1, d = tid & 127;
      f2 ml[8];
      float M = -INFINITY;
#pragma unroll
      for (int k = 0; k < 8; ++k) { ml[k] = *(const LAS f2*)(L + L_MLS + k * 16 + hh * 8); M = fmaxf(M, ml[k].x); }
      float o = 0.f, Ls = 0.f;
#pragma unroll
      for (int k = 0; k < 8; ++k) { const float f = ex2(ml[k].x - M); o = fmaf(f, LDF(L_MRG + k * 1024 + d * 8 + hh * 4), o); Ls = fmaf(f, ml[k].y, Ls); }
      st8(rs, WS_AP + u * 2048 + tid * 8, v2u{fu(o), tag});
      if (d == 0) st16(rs, WS_AM + u * 128 + hh * 16, v4u{fu(M), tag, fu(Ls), tag});
    }
    ST(8);
    vmwait<0>();
    if (w == 0) {
      v4u v[4];
      const int p0 = (u & ~7) + g, hh = h & 1;
      const int n = sweep<4>(rs, tag, v, [&](int k) {
        return k < 2 ? WS_AP + (p0 + 4 * k) * 2048 + hh * 1024 + q4 * 256 + j16 * 16 : WS_AM + (p0 + 4 * (k - 2)) * 128 + hh * 16; });
      STN(17, n);
      float r[2][2];
#pragma unroll
      for (int b = 0; b < 2; ++b) {
        const float m = uf(v[2 + b].x), M = sw32max(sw16max(m)), f = ex2(m - M);
        float Ls = f * uf(v[2 + b].z);
        Ls = sw16add(Ls, Ls); Ls = sw32add(Ls, Ls);
        const float s = f * __builtin_amdgcn_rcpf(Ls);
        r[b][0] = s * uf(v[b].x); r[b][1] = s * uf(v[b].z);
      }
      float e0 = sw32add(r[0][0], r[1][0]), e1 = sw32add(r[0][1], r[1][1]);
      e0 = sw16add(e0, e0); e1 = sw16add(e1, e1);
      *(LAS unsigned*)(L + L_OV + (lane >> 5) * 64 + j16 * 4) = pk(e0, e1);
      ST(9);
    }
    bar();
    {
      const int ob = L_WA + (8 * g + tq) * 1152 + tp * 8;
#pragma unroll
      for (int n = 0; n < 4; ++n) wof[n] = cat(TR(ob + (4 * w + n) * 32), TR(ob + 4608 + (4 * w + n) * 32));
      wof[4] = cat(TR(ob + (32 + (w & 3)) * 32), TR(ob + 4608 + (32 + (w & 3)) * 32));
    }
  } else {
    const int bb = u >> 6;
    v4u qa[16];
    if (w < 4) {
      while (*(volatile LAS unsigned*)(L + L_FLAG) == 0u) {}
      asm volatile("" ::: "memory");
      const v4u qf = LD128(L_QV + (tq & 1) * 256 + tp * 64 + g * 16);
      float r2[2];
#pragma unroll
      for (int gr = 0; gr < 2; ++gr) {
        float s[4];
#pragma unroll
        for (int i = 0; i < 4; ++i) {
          const f4 d = mfma32(wuk[4 * gr + i], qf, f4{0.f, 0.f, 0.f, 0.f});
          s[i] = (tp & 2) ? ((tp & 1) ? d[3] : d[2]) : ((tp & 1) ? d[1] : d[0]);
        }
        const float a0 = ((tp & 1) ? s[1] : s[0]) + dpp<X1>((tp & 1) ? s[0] : s[1]);
        const float a1 = ((tp & 1) ? s[3] : s[2]) + dpp<X1>((tp & 1) ? s[2] : s[3]);
        r2[gr] = ((tp & 2) ? a1 : a0) + dpp<X2>((tp & 2) ? a0 : a1);
      }
      const float r = ((tq & 2) ? r2[1] : r2[0]) * C2;
      const float rn = dpp<X1>(r);
      if (!(tp & 1))
        st8(rs, WS_QA + (tq & 1) * 65536 + (8 * q4 + 2 * w + (tq >> 1)) * 2048 + h * 64 + ((g >> 1) ^ ((h >> 3) & 1)) * 32 + (g & 1) * 16 + (tp >> 1) * 8,
            v2u{pk(r, rn), tag});
      ST(4);
      vmwait<0>();
      const int base = WS_QA + bb * 65536 + w * 16384 + lane * 16;
      const int n = sweep<16>(rs, tag, qa, [&](int k) { return base + k * 1024; });
      STN(17, n);
    } else {
      const int jw = w - 4;
      const char* cp = a.ckv + (size_t)u * 131072 + jw * 1024;
#pragma unroll 4
      for (int i = 0; i < 32; ++i) {
        vmwait<B_W - 1>();
        dma_nt(LB + (4 * i + jw) * 1024, (lane ^ ((jw << 2) | (i & 3))) << 4, cp + i * 4096);
      }
    }
    ST(5);
    bar();
    if (w < 4) {
#pragma unroll
      for (int k = 0; k < 16; ++k) *(LAS v2u*)(L + L_QIMG + w * 8192 + k * 512 + lane * 8) = v2u{qa[k].x, qa[k].z};
    }
    vmwait<0>();
    bar();
    ST(6);
    const int hd = lane & 31, hh = lane >> 5;
    v4u tw[17];
    f16v X;
    float mloc = 0.f;
    if (w < 4) {
      const int fr = ((hd & 3) << 2) | ((hd >> 2) & 3);
      const int ab = (32 * w + hd) * 1024, qb = L_QIMG + hd * 32 + (hh ^ ((hd >> 3) & 1)) * 16;
#pragma unroll
      for (int r = 0; r < 16; ++r) X[r] = 0.f;
#pragma unroll
      for (int s = 0; s < 32; ++s)
        X = mfmaB(LD128(ab + (s >> 3) * 256 + (((2 * (s & 7) + hh) ^ fr) << 4)), LD128(qb + s * 1024), X);
      float m = X[0];
#pragma unroll
      for (int r = 1; r < 16; ++r) m = fmaxf(m, X[r]);
      mloc = sw32max(m);
    } else {
      const int jw = w - 4;
      const rsrc_t rv = __builtin_amdgcn_make_buffer_rsrc((void*)a.wuv, 0, 4194304, 0x00020000);
      const rsrc_t ro = __builtin_amdgcn_make_buffer_rsrc((void*)a.wo[1], 0, 4718592, 0x00020000);
      const int o = (128 * q4 + (jw >> 1) * 64 + (jw & 1) * 4 + g) * 8192 + h * 256 + ((j16 ^ (g << 1)) << 4);
#pragma unroll
      for (int i = 0; i < 8; ++i) tw[i] = __builtin_amdgcn_raw_buffer_load_b128(rv, o, i * 8 * 8192, 2);
#pragma unroll
      for (int i = 0; i < 9; ++i) tw[8 + i] = __builtin_amdgcn_raw_buffer_load_b128(ro, u * 36864 + jw * 9216 + lane * 16, i * 1024, 2);
    }
    bar();
    if (w < 4 && lane < 32) *(LAS float*)(L + L_MX + w * 128 + lane * 4) = mloc;
    bar();
    float Mx = 0.f;
    if (w < 4) {
      Mx = fmaxf(fmaxf(LDF(L_MX + hd * 4), LDF(L_MX + 128 + hd * 4)), fmaxf(LDF(L_MX + 256 + hd * 4), LDF(L_MX + 384 + hd * 4)));
      float l = 0.f;
#pragma unroll
      for (int r = 0; r < 16; ++r) { X[r] = ex2(X[r] - Mx); l += X[r]; }
#pragma unroll
      for (int s = 0; s < 2; ++s)
        *(LAS v4u*)(L + L_PF + (2 * w + s) * 1024 + lane * 16) =
            v4u{pk(X[8 * s], X[8 * s + 1]), pk(X[8 * s + 2], X[8 * s + 3]), pk(X[8 * s + 4], X[8 * s + 5]), pk(X[8 * s + 6], X[8 * s + 7])};
      l = sw32add(l, l);
      if (lane < 32) *(LAS float*)(L + L_LS + w * 128 + lane * 4) = l;
    }
    bar();
    ST(7);
    {
      f16v O[2];
#pragma unroll
      for (int r = 0; r < 16; ++r) { O[0][r] = 0.f; O[1][r] = 0.f; }
      const int cb = (w >> 1) * 256, c0 = (w & 1) * 8 + 2 * (g & 1) + (tp >> 1);
      int tb[2][2];
#pragma unroll
      for (int ct = 0; ct < 2; ++ct)
#pragma unroll
        for (int tt = 0; tt < 2; ++tt) tb[ct][tt] = (8 * tt + 4 * hh + tq) * 1024 + cb + (((c0 + 4 * ct) ^ ((tq << 2) | (2 * tt + hh))) << 4) + (tp & 1) * 8;
#pragma unroll
      for (int ks = 0; ks < 8; ++ks) {
        const v4u p = LD128(L_PF + ks * 1024 + lane * 16);
#pragma unroll
        for (int ct = 0; ct < 2; ++ct) O[ct] = mfmaB(cat(TR(tb[ct][0] + ks * 16384), TR(tb[ct][1] + ks * 16384)), p, O[ct]);
      }
      const int fr = ((hd & 3) << 2) | ((hd >> 2) & 3);
#pragma unroll
      for (int n = 0; n < 4; ++n) {
        const f16v& o = O[n >> 1];
        const int r = (n & 1) * 8;
        *(LAS v4u*)(L + hd * 1024 + cb + ((((w & 1) * 8 + 4 * hh + n) ^ fr) << 4)) =
            v4u{pk(o[r], o[r + 1]), pk(o[r + 2], o[r + 3]), pk(o[r + 4], o[r + 5]), pk(o[r + 6], o[r + 7])};
      }
      asm volatile("s_waitcnt lgkmcnt(0)" ::: "memory");
#pragma unroll
      for (int k = 0; k < 4; ++k) {
        const int r = 8 * k + (lane >> 3), f2_ = ((r & 3) << 2) | ((r >> 2) & 3);
        st16(rs, WS_BP + (r * 4 + (w >> 1)) * 32768 + u * 256 + (w & 1) * 128 + (lane & 7) * 16,
             LD128(r * 1024 + cb + ((((w & 1) * 8 + (lane & 7)) ^ f2_) << 4)));
      }
    }
    ST(8);
    vmwait<0>();
    bar();
    ST(9);
    v4u ml[2];
    if (w == 0) {
      if (lane < 32) {
        const float Ls = (LDF(L_LS + lane * 4) + LDF(L_LS + 128 + lane * 4)) + (LDF(L_LS + 256 + lane * 4) + LDF(L_LS + 384 + lane * 4));
        st16(rs, WS_ML + lane * 2048 + u * 16, v4u{fu(Mx), tag, fu(Ls), tag});
      }
      vmwait<0>();
      const int n = sweep<2>(rs, tag, ml, [&](int k) { return WS_ML + h * 2048 + k * 1024 + lane * 16; });
      STN(18, n);
    } else if (w >= 4) {
      const int jw = w - 4;
#pragma unroll
      for (int i = 0; i < 8; ++i) *(LAS v4u*)(L + L_WUV + (8 * jw + i) * 1152 + lane * 16) = tw[i];
#pragma unroll
      for (int i = 0; i < 9; ++i) *(LAS v4u*)(L + L_WOB + (9 * jw + i) * 1024 + lane * 16) = tw[8 + i];
    }
    bar();
    ST(10);
    v4u bp[4];
#pragma unroll
    for (int k = 0; k < 4; ++k) bp[k] = ld16(rs, WS_BP + u * 32768 + k * 8192 + tid * 16);
    asm volatile("" ::: "memory");
    if (w == 0) {
#pragma unroll
      for (int b = 0; b < 2; ++b) {
        const float m = uf(ml[b].x);
        float M = fmaxf(m, dpp<X1>(m)); M = fmaxf(M, dpp<X2>(M)); M = fmaxf(M, dpp<XH>(M)); M = fmaxf(M, dpp<X8>(M));
        M = sw32max(sw16max(M));
        const float f = ex2(m - M);
        float Ls = f * uf(ml[b].z);
        Ls += dpp<X1>(Ls); Ls += dpp<X2>(Ls); Ls += dpp<XH>(Ls); Ls += dpp<X8>(Ls);
        Ls = sw16add(Ls, Ls); Ls = sw32add(Ls, Ls);
        *(LAS float*)(L + L_F + b * 256 + lane * 4) = f * __builtin_amdgcn_rcpf(Ls);
      }
    }
    v4u uvf[4];
    {
      const int ub = L_WUV + 2 * g * 1152 + tq * 256 + (((2 * w + (tp >> 1)) ^ (tq << 1)) << 4) + (tp & 1) * 8;
#pragma unroll
      for (int s = 0; s < 4; ++s) uvf[s] = cat(TR(ub + s * 9216), TR(ub + s * 9216 + 1152));
      const int ob = L_WOB + (8 * g + tq) * 1152 + tp * 8;
#pragma unroll
      for (int n = 0; n < 4; ++n) wof[n] = cat(TR(ob + (4 * w + n) * 32), TR(ob + 4608 + (4 * w + n) * 32));
      wof[4] = cat(TR(ob + (32 + (w & 3)) * 32), TR(ob + 4608 + (32 + (w & 3)) * 32));
    }
    bar();
    {
      float acc[2][8];
#pragma unroll
      for (int b = 0; b < 2; ++b) {
        const float f0 = LDF(L_F + (64 * b + 4 * w + g) * 4), f1 = LDF(L_F + (64 * b + 32 + 4 * w + g) * 4);
#pragma unroll
        for (int e = 0; e < 4; ++e) {
          acc[b][2 * e] = fmaf(f1, lo16(bp[2 * b + 1][e]), f0 * lo16(bp[2 * b][e]));
          acc[b][2 * e + 1] = fmaf(f1, hi16(bp[2 * b + 1][e]), f0 * hi16(bp[2 * b][e]));
        }
      }
      ST(11);
      float t[8], r[4];
#pragma unroll
      for (int e = 0; e < 8; ++e) t[e] = sw32add(acc[0][e], acc[1][e]);
#pragma unroll
      for (int e = 0; e < 4; ++e) r[e] = sw16add(t[e], t[e + 4]);
      *(LAS v2u*)(L + L_CC + (2 * w + hh) * 272 + j16 * 16 + (g & 1) * 8) = v2u{pk(r[0], r[1]), pk(r[2], r[3])};
    }
    bar();
    {
      f4 d = {0.f, 0.f, 0.f, 0.f};
#pragma unroll
      for (int s = 0; s < 4; ++s) d = mfma32(LD128(L_CC + j16 * 272 + s * 64 + g * 16), uvf[s], d);
      float e = sw32add(d[0] + d[2], d[1] + d[3]);
      e = sw16add(e, e);
      if (!(g & 1)) st8(rs, WS_CX + u * 2048 + hh * 1024 + (16 * w + j16) * 8, v2u{fu(e), tag});
    }
    ST(12);
    if (w == 0) {
      vmwait<0>();
      v4u v[2];
      sweep<2>(rs, tag, v, [&](int k) { return WS_CX + (4 * h + g) * 2048 + k * 1024 + q4 * 256 + j16 * 16; });
      float e0 = sw32add(uf(v[0].x), uf(v[1].x)), e1 = sw32add(uf(v[0].z), uf(v[1].z));
      e0 = sw16add(e0, e0); e1 = sw16add(e1, e1);
      *(LAS unsigned*)(L + L_OV + hh * 64 + j16 * 4) = pk(e0, e1);
      ST(13);
    }
    bar();
  }
  {
    const v4u of = LD128(L_OV + (lane & 1) * 64 + g * 16);
    f4 d[5];
#pragma unroll
    for (int n = 0; n < 5; ++n) d[n] = mfma32(of, wof[n], f4{0.f, 0.f, 0.f, 0.f});
    const float y0 = (g & 2) ? ((g & 1) ? d[3][0] : d[2][0]) : ((g & 1) ? d[1][0] : d[0][0]);
    const float y1 = (g & 2) ? ((g & 1) ? d[3][1] : d[2][1]) : ((g & 1) ? d[1][1] : d[0][1]);
    const int ob = WS_OP + role * 72 * 16384 + u * 128 + (lane & 7) * 16;
    st16(rs, ob + (8 * w + (lane >> 3)) * 16384, v4u{fu(y0), tag, fu(y1), tag});
    if (w >= 4 && lane < 16) st16(rs, ob + (64 + 2 * (w & 3) + (lane >> 3)) * 16384, v4u{fu(d[4][0]), tag, fu(d[4][1]), tag});
  }
  if (role) ST(14); else ST(10);
  if (u < 72) {
    vmwait<0>();
    v4u v[2];
    const int n = sweep<2>(rs, tag, v, [&](int k) { return WS_OP + (role * 72 + u) * 16384 + w * 2048 + k * 1024 + lane * 16; });
    if (role) STN(19, n); else STN(18, n);
    if (!role) ST(11);
    float e = sw32add(uf(v[0].x) + uf(v[1].x), uf(v[0].z) + uf(v[1].z));
    e = sw16add(e, e);
    e += dpp<X8>(e);
    if ((lane & 24) == 0) *(LAS float*)(L + L_RED + w * 64 + (lane >> 5) * 32 + (lane & 7) * 4) = e;
    bar();
    if (tid < 16) {
      float s = 0.f;
#pragma unroll
      for (int k = 0; k < 8; ++k) s += LDF(L_RED + k * 64 + tid * 4);
      *(GAS unsigned short*)(a.out + ((tid >> 3) * 1152 + role * 576 + 8 * u + (tid & 7)) * 2) = (unsigned short)pk(s, s);
    }
  } else if (c == 255) {
    if (w < 4) {
      for (int n = 0; n < SPIN_CAP; ++n) {
        asm volatile("" ::: "memory");
        const v2u v = ld8(rs, WS_QP + tid * 2048);
        if (__builtin_amdgcn_ballot_w64(v.y != tag) == 0) break;
        __builtin_amdgcn_s_sleep(2);
      }
    }
    bar();
    if (tid == 0) __builtin_amdgcn_raw_buffer_store_b32(tag, rs, WS_E, 0, 16);
  }
  if (role) ST(15); else ST(12);
}

extern "C" void kernel_launch(void* const* d_in, const int* in_sizes, int n_in, void* d_out, int out_size, void* d_ws, size_t ws_size, hipStream_t stream) {
  Args a;
  a.x[0] = (const char*)d_in[0]; a.x[1] = (const char*)d_in[5];
  a.wq[0] = (const char*)d_in[3]; a.wq[1] = (const char*)d_in[7];
  a.wo[0] = (const char*)d_in[4]; a.wo[1] = (const char*)d_in[10];
  a.kc = (const char*)d_in[1]; a.vc = (const char*)d_in[2]; a.ckv = (const char*)d_in[6];
  a.wuk = (const char*)d_in[8]; a.wuv = (const char*)d_in[9];
  a.out = (char*)d_out; a.ws = (char*)d_ws;
  fused_decode<<<256, 512, 0, stream>>>(a);
}
```

```cpp
#define STAMP
#define PROBE_ROLE 1
#define PROBE_K 15
#define PROBE_K0 0
#define PROBE_MULT 0
#include <hip/hip_runtime.h>
#include <stdint.h>

typedef unsigned v4u __attribute__((ext_vector_type(4)));
typedef unsigned v2u __attribute__((ext_vector_type(2)));
typedef float f2 __attribute__((ext_vector_type(2)));
typedef float f4 __attribute__((ext_vector_type(4)));
typedef float f16v __attribute__((ext_vector_type(16)));
typedef short s4 __attribute__((ext_vector_type(4)));
typedef __bf16 b2 __attribute__((ext_vector_type(2)));
typedef __bf16 b8 __attribute__((ext_vector_type(8)));
#define LAS __attribute__((address_space(3)))
#define GAS __attribute__((address_space(1)))
#define DI __device__ __forceinline__

#define A_W0 2
#define A_NSLOW 3
#define A_W 4
#define B_W 6

#define CODE_KB 15
constexpr float C2 = 0.08838834764831845f * 1.4426950408889634f;
constexpr int SPIN_CAP = 1 << 16;
constexpr int WS_E = 0, WS_QP = 4096, WS_QA = WS_QP + 524288, WS_ML = WS_QA + 131072, WS_CX = WS_ML + 65536,
              WS_AP = WS_CX + 262144, WS_AM = WS_AP + 262144, WS_OP = WS_AM + 16384, WS_BP = WS_OP + 2359296,
              WS_END = WS_BP + 4194304;
constexpr int L_WA = 110592, L_WQB = 89600, L_QIMG = 131072, L_SCR = 152064;
constexpr int L_X = L_SCR, L_QV = L_SCR + 640, L_FLAG = L_SCR + 1152, L_DUMMY = L_SCR + 1280, L_MRG = L_SCR + 2304,
              L_MLS = L_SCR + 10496, L_OV = L_SCR + 10624, L_RED = L_SCR + 10752;
constexpr int L_MX = L_QIMG, L_LS = L_QIMG + 512, L_PF = L_QIMG + 1024, L_F = L_QIMG + 12288, L_CC = L_QIMG + 13312;
constexpr int L_WUV = 0, L_WOB = 36864;

constexpr int NSTAMP = 20, NSTAMP_T = 16, STAMP_OFF = 16 << 20;
#define ROLE_OF(c) (((c) >> 3) & 1)
static const char* STAMP_NAME[2][NSTAMP] = {
  {"start", "wq landed", "qp published", "q gathered", "q in regs", "QK done", "lastV landed", "PV done", "ap published", "ap gathered", "op published", "op gathered", "end", "", "", "", "polls q", "polls ap", "polls op", ""},
  {"start", "wq landed", "qp published", "q gathered", "qa published", "qa swept/iss", "attn start", "QK done", "PV done", "bp drained", "ml gathered", "bp landed", "cx published", "cx gathered", "op published", "end", "polls q", "polls qa", "polls ml", "polls op"}};
#define ST(k) do { unsigned long long t_; asm volatile("s_memrealtime %0\n\ts_waitcnt lgkmcnt(0)" : "=s"(t_) :: "memory"); stp[k] = (unsigned)t_ | 1u; } while (0)
#define STN(k, n) do { stp[k] = (n); } while (0)

struct Args {
  const char* x[2]; const char* wq[2]; const char* wo[2];
  const char* kc; const char* vc; const char* ckv; const char* wuk; const char* wuv;
  char* out; char* ws;
};

template <int N> DI void vmwait() { asm volatile("s_waitcnt vmcnt(%0) lgkmcnt(0)" ::"n"(N) : "memory"); }
DI void bar() { asm volatile("s_waitcnt lgkmcnt(0)\n\ts_barrier" ::: "memory"); }
DI void dma_nt(unsigned lds, unsigned voff, const char* base) {
  unsigned keep;
  asm volatile("s_mov_b32 %0, m0\n\ts_mov_b32 m0, %1\n\ts_nop 2\n\tglobal_load_lds_dwordx4 %2, %3 nt\n\ts_mov_b32 m0, %0"
               : "=&s"(keep) : "s"(lds), "v"(voff), "s"(base) : "memory");
}
DI void dma_def(unsigned lds, unsigned voff, const char* base) {
  unsigned keep;
  asm volatile("s_mov_b32 %0, m0\n\ts_mov_b32 m0, %1\n\ts_nop 2\n\tglobal_load_lds_dwordx4 %2, %3\n\ts_mov_b32 m0, %0"
               : "=&s"(keep) : "s"(lds), "v"(voff), "s"(base) : "memory");
}
typedef __amdgpu_buffer_rsrc_t rsrc_t;
DI v4u ld16(rsrc_t rs, int off) { return __builtin_amdgcn_raw_buffer_load_b128(rs, off, 0, 16); }
DI v2u ld8(rsrc_t rs, int off) { return __builtin_amdgcn_raw_buffer_load_b64(rs, off, 0, 16); }
DI void st16(rsrc_t rs, int off, v4u v) { __builtin_amdgcn_raw_buffer_store_b128(v, rs, off, 0, 16); }
DI void st8(rsrc_t rs, int off, v2u v) { __builtin_amdgcn_raw_buffer_store_b64(v, rs, off, 0, 16); }
DI unsigned fu(float x) { return __float_as_uint(x); }
DI float uf(unsigned x) { return __uint_as_float(x); }
DI unsigned pk(float lo, float hi) { return __builtin_bit_cast(unsigned, __builtin_convertvector(f2{lo, hi}, b2)); }
DI float lo16(unsigned x) { return uf(x << 16); }
DI float hi16(unsigned x) { return uf(x & 0xffff0000u); }
template <int CTRL> DI float dpp(float x) { return uf(__builtin_amdgcn_mov_dpp(fu(x), CTRL, 0xf, 0xf, true)); }
constexpr int X1 = 0xB1, X2 = 0x4E, XH = 0x141, X8 = 0x128;
DI float sw32add(float a, float b) { auto r = __builtin_amdgcn_permlane32_swap(fu(a), fu(b), false, false); return uf(r[0]) + uf(r[1]); }
DI float sw16add(float a, float b) { auto r = __builtin_amdgcn_permlane16_swap(fu(a), fu(b), false, false); return uf(r[0]) + uf(r[1]); }
DI float sw32max(float a) { auto r = __builtin_amdgcn_permlane32_swap(fu(a), fu(a), false, false); return fmaxf(uf(r[0]), uf(r[1])); }
DI float sw16max(float a) { auto r = __builtin_amdgcn_permlane16_swap(fu(a), fu(a), false, false); return fmaxf(uf(r[0]), uf(r[1])); }
DI f4 mfma32(v4u a, v4u b, f4 c) { return __builtin_amdgcn_mfma_f32_16x16x32_bf16(__builtin_bit_cast(b8, a), __builtin_bit_cast(b8, b), c, 0, 0, 0); }
DI f4 mfma16(v2u a, v2u b, f4 c) { return __builtin_amdgcn_mfma_f32_16x16x16bf16_1k(__builtin_bit_cast(s4, a), __builtin_bit_cast(s4, b), c, 0, 0, 0); }
DI f16v mfmaB(v4u a, v4u b, f16v c) { return __builtin_amdgcn_mfma_f32_32x32x16_bf16(__builtin_bit_cast(b8, a), __builtin_bit_cast(b8, b), c, 0, 0, 0); }
DI v4u cat(v2u a, v2u b) { return v4u{a.x, a.y, b.x, b.y}; }
DI float ex2(float x) { return __builtin_amdgcn_exp2f(x); }

template <int N, class F> DI int sweep(rsrc_t rs, unsigned tag, v4u (&v)[N], F off) {
  int n = 0;
  for (;;) {
    asm volatile("" ::: "memory");
#pragma unroll
    for (int k = 0; k < N; ++k) v[k] = ld16(rs, off(k));
    unsigned bad = 0;
#pragma unroll
    for (int k = 0; k < N; ++k) bad |= (v[k].y ^ tag) | (v[k].w ^ tag);
    ++n;
    if (__builtin_amdgcn_ballot_w64(bad != 0) == 0 || n > SPIN_CAP) break;
    __builtin_amdgcn_s_sleep(2);
  }
  return n;
}

__global__ void __launch_bounds__(512, 2) fused_decode(Args a) {
  __shared__ __attribute__((aligned(16))) char smem[163840];
  LAS char* const L = (LAS char*)smem;
  const unsigned LB = (unsigned)(uintptr_t)L;
#define LD128(o) (*(const LAS v4u*)(L + (o)))
#define LD64(o) (*(const LAS v2u*)(L + (o)))
#define LDF(o) (*(const LAS float*)(L + (o)))
#define TR(o) __builtin_bit_cast(v2u, __builtin_amdgcn_ds_read_tr16_b64_v4i16((LAS s4*)(L + (o))))
  unsigned stp[NSTAMP];
#pragma unroll
  for (int k = 0; k < NSTAMP; ++k) stp[k] = 0;
  ST(0);
  const int c = blockIdx.x, tid = threadIdx.x, lane = tid & 63;
  const int w = __builtin_amdgcn_readfirstlane(tid >> 6);
  const int role = (c >> 3) & 1, u = (c & 7) * 16 + (c >> 4), h = u >> 2, q4 = u & 3;
  const int g = lane >> 4, j16 = lane & 15, tq = (lane >> 2) & 3, tp = lane & 3;

  unsigned ev;
  { const unsigned z = 0; asm volatile("s_nop 4\n\tglobal_load_dword %0, %1, %2 sc1" : "=v"(ev) : "v"(z), "s"(a.ws) : "memory"); }
  const unsigned voffV = g * 256 + ((j16 ^ (g << 1)) << 4);
  const int lwq = role ? L_WQB : L_WA;
  {
    const char* wq = a.wq[role] + (size_t)(144 * q4) * 8192 + h * 256;
    const unsigned vo = g * 8192 + ((j16 ^ (g << 1)) << 4);
    if (w == 0) { if (lane < 36) dma_def(LB + L_X, (lane >= 18 ? 1152 - 288 : 0) + lane * 16, a.x[role] + 288 * q4); }
    if (w == 1) {
      unsigned long long pc; asm volatile("s_getpc_b64 %0" : "=s"(pc));
      const int ch = c >> 3;
      if (ch < CODE_KB) dma_def(LB + L_DUMMY, lane * 16, (const char*)(pc & ~255ull) + ch * 1024);
    }
    if (tid == 128) *(LAS unsigned*)(L + L_FLAG) = 0u;
#pragma unroll
    for (int i = 0; i < 4; ++i) {
      if (i >= A_W0 && !role) vmwait<A_W0 - 1>();
      dma_nt(LB + lwq + (w + 8 * i) * 1152, vo, wq + (size_t)(w + 8 * i) * 32768);
    }
    if (4 >= A_W0 && !role) vmwait<A_W0 - 1>();
    const int rl = 2 * (w & 1) + g;
    if (lane < 32) dma_nt(LB + lwq + (32 + (w >> 1)) * 1152 + (w & 1) * 512, rl * 8192 + ((j16 ^ (rl << 1)) << 4), wq + (size_t)(32 + (w >> 1)) * 32768);
  }
  const rsrc_t rs = __builtin_amdgcn_make_buffer_rsrc(a.ws, 0, WS_END, 0x00020000);
  v4u wuk[8];
  if (role && w < 4) {
    const rsrc_t ru = __builtin_amdgcn_make_buffer_rsrc((void*)a.wuk, 0, 4194304, 0x00020000);
    const int o = (128 * q4 + 32 * w + 4 * tq) * 8192 + h * 256 + (4 * tp + g) * 16;
#pragma unroll
    for (int i = 0; i < 8; ++i) wuk[i] = __builtin_amdgcn_raw_buffer_load_b128(ru, o, ((i >> 2) * 16 + (i & 3)) * 8192, 2);
    vmwait<8>();
  } else {
    vmwait<0>();
  }
  asm volatile("" : "+v"(ev));
  const unsigned tag = __builtin_amdgcn_readfirstlane(ev) + 1u;
  bar();
  ST(1);
  {
    const int tb = lwq + g * 1152 + tq * 256 + (((2 * w + (tp >> 1)) ^ (tq << 1)) << 4) + (tp & 1) * 8;
    const int xb = L_X + (lane & 1) * 288 + g * 8;
    f4 acc = {0.f, 0.f, 0.f, 0.f};
#pragma unroll
    for (int t = 0; t < 8; t += 2)
      acc = mfma32(cat(LD64(xb + t * 32), LD64(xb + t * 32 + 32)), cat(TR(tb + t * 4608), TR(tb + t * 4608 + 4608)), acc);
    acc += mfma16(LD64(xb + 256), TR(tb + 8 * 4608), f4{0.f, 0.f, 0.f, 0.f});
    if (lane < 32) st8(rs, WS_QP + (role * 128 + u) * 2048 + g * 1024 + (16 * w + j16) * 8, v2u{fu(g ? acc[1] : acc[0]), tag});
  }
  bar();
  ST(2);
  if (w == 0) {
    vmwait<0>();
    v4u v[8];
    const int base = WS_QP + lane * 16 + (role ? (128 + 4 * h) * 2048 : (u & ~7) * 2048 + ((u >> 2) & 1) * 1024);
    const int vs = role ? 1024 : 8192;
    const int n = sweep<8>(rs, tag, v, [&](int k) { return base + (k >> 2) * vs + (k & 3) * 2048; });
    STN(16, n);
#pragma unroll
    for (int k = 0; k < 2; ++k)
      *(LAS unsigned*)(L + L_QV + k * 256 + lane * 4) =
          pk((uf(v[4 * k].x) + uf(v[4 * k + 1].x)) + (uf(v[4 * k + 2].x) + uf(v[4 * k + 3].x)),
             (uf(v[4 * k].z) + uf(v[4 * k + 1].z)) + (uf(v[4 * k + 2].z) + uf(v[4 * k + 3].z)));
    asm volatile("s_waitcnt lgkmcnt(0)" ::: "memory");
    *(volatile LAS unsigned*)(L + L_FLAG) = 1u;
    ST(3);
  }
  v4u wof[5];
  if (!role) {
    const size_t kvo = ((size_t)(((u >> 2) & 1) * 16 + (u >> 3)) * 8192 + 6144 + 512 * q4 + 16 * w) * 256;
    const char* kp = a.kc + kvo;
    const char* vp = a.vc + kvo;
    const char* wo = a.wo[0] + (size_t)u * 36864 + w * 4608;
    const unsigned voffK = g * 256 + ((j16 ^ g) << 4);
    const int ring = w * 13824;
    const int kb = ring + tq * 1088 + tp * 256 + ((g ^ tp) << 4);
    const int vb = ring + g * 1152 + tq * 256 + (tp & 1) * 8;
    v4u qf[4];
    f4 sc[4], oc[8];
    v2u pf[4];
    float mw = 0.f, lw = 0.f;
#pragma unroll
    for (int n = 0; n < 8; ++n) oc[n] = f4{0.f, 0.f, 0.f, 0.f};
#pragma unroll
    for (int n = 0; n <= 31 + A_W; ++n) {
      if (n < A_NSLOW) vmwait<A_W0 - 1>(); else if (n < 37) vmwait<A_W - 1>(); else vmwait<5>();
#pragma unroll
      for (int m = n; m < (n == 31 + A_W ? 37 : n + 1); ++m) {
        const int T = m >> 2, i = m & 3;
        if (m < 16) dma_nt(LB + ring + (T % 3) * 4608 + i * 1088, voffK, kp + T * 32768 + i * 1024);
        else if (m < 32) dma_nt(LB + ring + (T % 3) * 4608 + i * 1152, voffV, vp + (T - 4) * 32768 + i * 1024);
        else if (m < 36) dma_nt(LB + L_WA + w * 4608 + i * 1024, lane * 16, wo + i * 1024);
        else if (m == 36 && lane < 32) dma_nt(LB + L_WA + w * 4608 + 4096, lane * 16, wo + 4096);
      }
      const int d = n - 3 - A_W;
      if (d >= 0 && (d & 3) == 0) {
        const int T = d >> 2;
        if (T == 0) {
          while (*(volatile LAS unsigned*)(L + L_FLAG) == 0u) __builtin_amdgcn_s_sleep(1);
          asm volatile("" ::: "memory");
#pragma unroll
          for (int s = 0; s < 4; ++s) qf[s] = LD128(L_QV + (lane & 1) * 256 + s * 64 + g * 16);
          ST(4);
        }
        if (T < 4) {
          f4 s4_ = {0.f, 0.f, 0.f, 0.f};
#pragma unroll
          for (int s = 0; s < 4; ++s) s4_ = mfma32(LD128(kb + (T % 3) * 4608 + s * 64), qf[s], s4_);
          sc[T] = s4_;
          if (T == 3) {
            float m = -INFINITY;
#pragma unroll
            for (int t = 0; t < 4; ++t) m = fmaxf(fmaxf(fmaxf(sc[t][0], sc[t][1]), fmaxf(sc[t][2], sc[t][3])), m);
            m = sw32max(sw16max(m));
            mw = m * C2;
            float l = 0.f;
#pragma unroll
            for (int t = 0; t < 4; ++t) {
              const float p0 = ex2(fmaf(sc[t][0], C2, -mw)), p1 = ex2(fmaf(sc[t][1], C2, -mw)), p2 = ex2(fmaf(sc[t][2], C2, -mw)), p3 = ex2(fmaf(sc[t][3], C2, -mw));
              l += (p0 + p1) + (p2 + p3);
              pf[t] = v2u{pk(p0, p1), pk(p2, p3)};
            }
            l = sw16add(l, l);
            lw = sw32add(l, l);
            ST(5);
          }
        } else {
          if (T == 7) ST(6);
#pragma unroll
          for (int n2 = 0; n2 < 8; ++n2)
            oc[n2] = mfma16(pf[T - 4], TR(vb + (T % 3) * 4608 + (((2 * n2 + (tp >> 1)) ^ (tq << 1)) << 4)), oc[n2]);
        }
      }
    }
    if (lane < 16) {
#pragma unroll
      for (int n = 0; n < 8; ++n) *(LAS f2*)(L + L_MRG + w * 1024 + (16 * n + lane) * 8) = f2{oc[n][0], oc[n][1]};
      if (lane < 2) *(LAS f2*)(L + L_MLS + w * 16 + lane * 8) = f2{mw, lw};
    }
    ST(7);
    bar();
    if (w < 4) {
      const int hh = w >> 1, d = tid & 127;
      f2 ml[8];
      float M = -INFINITY;
#pragma unroll
      for (int k = 0; k < 8; ++k) { ml[k] = *(const LAS f2*)(L + L_MLS + k * 16 + hh * 8); M = fmaxf(M, ml[k].x); }
      float o = 0.f, Ls = 0.f;
#pragma unroll
      for (int k = 0; k < 8; ++k) { const float f = ex2(ml[k].x - M); o = fmaf(f, LDF(L_MRG + k * 1024 + d * 8 + hh * 4), o); Ls = fmaf(f, ml[k].y, Ls); }
      st8(rs, WS_AP + u * 2048 + tid * 8, v2u{fu(o), tag});
      if (d == 0) st16(rs, WS_AM + u * 128 + hh * 16, v4u{fu(M), tag, fu(Ls), tag});
    }
    ST(8);
    vmwait<0>();
    if (w == 0) {
      v4u v[4];
      const int p0 = (u & ~7) + g, hh = h & 1;
      const int n = sweep<4>(rs, tag, v, [&](int k) {
        return k < 2 ? WS_AP + (p0 + 4 * k) * 2048 + hh * 1024 + q4 * 256 + j16 * 16 : WS_AM + (p0 + 4 * (k - 2)) * 128 + hh * 16; });
      STN(17, n);
      float r[2][2];
#pragma unroll
      for (int b = 0; b < 2; ++b) {
        const float m = uf(v[2 + b].x), M = sw32max(sw16max(m)), f = ex2(m - M);
        float Ls = f * uf(v[2 + b].z);
        Ls = sw16add(Ls, Ls); Ls = sw32add(Ls, Ls);
        const float s = f * __builtin_amdgcn_rcpf(Ls);
        r[b][0] = s * uf(v[b].x); r[b][1] = s * uf(v[b].z);
      }
      float e0 = sw32add(r[0][0], r[1][0]), e1 = sw32add(r[0][1], r[1][1]);
      e0 = sw16add(e0, e0); e1 = sw16add(e1, e1);
      *(LAS unsigned*)(L + L_OV + (lane >> 5) * 64 + j16 * 4) = pk(e0, e1);
      ST(9);
    }
    bar();
    {
      const int ob = L_WA + (8 * g + tq) * 1152 + tp * 8;
#pragma unroll
      for (int n = 0; n < 4; ++n) wof[n] = cat(TR(ob + (4 * w + n) * 32), TR(ob + 4608 + (4 * w + n) * 32));
      wof[4] = cat(TR(ob + (32 + (w & 3)) * 32), TR(ob + 4608 + (32 + (w & 3)) * 32));
    }
  } else {
    const int bb = u >> 6;
    v4u qa[16];
    if (w < 4) {
      while (*(volatile LAS unsigned*)(L + L_FLAG) == 0u) {}
      asm volatile("" ::: "memory");
      const v4u qf = LD128(L_QV + (tq & 1) * 256 + tp * 64 + g * 16);
      float r2[2];
#pragma unroll
      for (int gr = 0; gr < 2; ++gr) {
        float s[4];
#pragma unroll
        for (int i = 0; i < 4; ++i) {
          const f4 d = mfma32(wuk[4 * gr + i], qf, f4{0.f, 0.f, 0.f, 0.f});
          s[i] = (tp & 2) ? ((tp & 1) ? d[3] : d[2]) : ((tp & 1) ? d[1] : d[0]);
        }
        const float a0 = ((tp & 1) ? s[1] : s[0]) + dpp<X1>((tp & 1) ? s[0] : s[1]);
        const float a1 = ((tp & 1) ? s[3] : s[2]) + dpp<X1>((tp & 1) ? s[2] : s[3]);
        r2[gr] = ((tp & 2) ? a1 : a0) + dpp<X2>((tp & 2) ? a0 : a1);
      }
      const float r = ((tq & 2) ? r2[1] : r2[0]) * C2;
      const float rn = dpp<X1>(r);
      if (!(tp & 1))
        st8(rs, WS_QA + (tq & 1) * 65536 + (8 * q4 + 2 * w + (tq >> 1)) * 2048 + h * 64 + ((g >> 1) ^ ((h >> 3) & 1)) * 32 + (g & 1) * 16 + (tp >> 1) * 8,
            v2u{pk(r, rn), tag});
      ST(4);
      vmwait<0>();
      const int base = WS_QA + bb * 65536 + w * 16384 + lane * 16;
      const int n = sweep<16>(rs, tag, qa, [&](int k) { return base + k * 1024; });
      STN(17, n);
    } else {
      const int jw = w - 4;
      const char* cp = a.ckv + (size_t)u * 131072 + jw * 1024;
#pragma unroll 4
      for (int i = 0; i < 32; ++i) {
        vmwait<B_W - 1>();
        dma_nt(LB + (4 * i + jw) * 1024, (lane ^ ((jw << 2) | (i & 3))) << 4, cp + i * 4096);
      }
    }
    ST(5);
    bar();
    if (w < 4) {
#pragma unroll
      for (int k = 0; k < 16; ++k) *(LAS v2u*)(L + L_QIMG + w * 8192 + k * 512 + lane * 8) = v2u{qa[k].x, qa[k].z};
    }
    vmwait<0>();
    bar();
    ST(6);
    const int hd = lane & 31, hh = lane >> 5;
    v4u tw[17];
    f16v X;
    float mloc = 0.f;
    if (w < 4) {
      const int fr = ((hd & 3) << 2) | ((hd >> 2) & 3);
      const int ab = (32 * w + hd) * 1024, qb = L_QIMG + hd * 32 + (hh ^ ((hd >> 3) & 1)) * 16;
#pragma unroll
      for (int r = 0; r < 16; ++r) X[r] = 0.f;
#pragma unroll
      for (int s = 0; s < 32; ++s)
        X = mfmaB(LD128(ab + (s >> 3) * 256 + (((2 * (s & 7) + hh) ^ fr) << 4)), LD128(qb + s * 1024), X);
      float m = X[0];
#pragma unroll
      for (int r = 1; r < 16; ++r) m = fmaxf(m, X[r]);
      mloc = sw32max(m);
    } else {
      const int jw = w - 4;
      const rsrc_t rv = __builtin_amdgcn_make_buffer_rsrc((void*)a.wuv, 0, 4194304, 0x00020000);
      const rsrc_t ro = __builtin_amdgcn_make_buffer_rsrc((void*)a.wo[1], 0, 4718592, 0x00020000);
      const int o = (128 * q4 + (jw >> 1) * 64 + (jw & 1) * 4 + g) * 8192 + h * 256 + ((j16 ^ (g << 1)) << 4);
#pragma unroll
      for (int i = 0; i < 8; ++i) tw[i] = __builtin_amdgcn_raw_buffer_load_b128(rv, o, i * 8 * 8192, 2);
#pragma unroll
      for (int i = 0; i < 9; ++i) tw[8 + i] = __builtin_amdgcn_raw_buffer_load_b128(ro, u * 36864 + jw * 9216 + lane * 16, i * 1024, 2);
    }
    bar();
    if (w < 4 && lane < 32) *(LAS float*)(L + L_MX + w * 128 + lane * 4) = mloc;
    bar();
    float Mx = 0.f;
    if (w < 4) {
      Mx = fmaxf(fmaxf(LDF(L_MX + hd * 4), LDF(L_MX + 128 + hd * 4)), fmaxf(LDF(L_MX + 256 + hd * 4), LDF(L_MX + 384 + hd * 4)));
      float l = 0.f;
#pragma unroll
      for (int r = 0; r < 16; ++r) { X[r] = ex2(X[r] - Mx); l += X[r]; }
#pragma unroll
      for (int s = 0; s < 2; ++s)
        *(LAS v4u*)(L + L_PF + (2 * w + s) * 1024 + lane * 16) =
            v4u{pk(X[8 * s], X[8 * s + 1]), pk(X[8 * s + 2], X[8 * s + 3]), pk(X[8 * s + 4], X[8 * s + 5]), pk(X[8 * s + 6], X[8 * s + 7])};
      l = sw32add(l, l);
      if (lane < 32) *(LAS float*)(L + L_LS + w * 128 + lane * 4) = l;
    }
    bar();
    ST(7);
    {
      f16v O[2];
#pragma unroll
      for (int r = 0; r < 16; ++r) { O[0][r] = 0.f; O[1][r] = 0.f; }
      const int cb = (w >> 1) * 256, c0 = (w & 1) * 8 + 2 * (g & 1) + (tp >> 1);
      int tb[2][2];
#pragma unroll
      for (int ct = 0; ct < 2; ++ct)
#pragma unroll
        for (int tt = 0; tt < 2; ++tt) tb[ct][tt] = (8 * tt + 4 * hh + tq) * 1024 + cb + (((c0 + 4 * ct) ^ ((tq << 2) | (2 * tt + hh))) << 4) + (tp & 1) * 8;
#pragma unroll
      for (int ks = 0; ks < 8; ++ks) {
        const v4u p = LD128(L_PF + ks * 1024 + lane * 16);
#pragma unroll
        for (int ct = 0; ct < 2; ++ct) O[ct] = mfmaB(cat(TR(tb[ct][0] + ks * 16384), TR(tb[ct][1] + ks * 16384)), p, O[ct]);
      }
      const int fr = ((hd & 3) << 2) | ((hd >> 2) & 3);
#pragma unroll
      for (int n = 0; n < 4; ++n) {
        const f16v& o = O[n >> 1];
        const int r = (n & 1) * 8;
        *(LAS v4u*)(L + hd * 1024 + cb + ((((w & 1) * 8 + 4 * hh + n) ^ fr) << 4)) =
            v4u{pk(o[r], o[r + 1]), pk(o[r + 2], o[r + 3]), pk(o[r + 4], o[r + 5]), pk(o[r + 6], o[r + 7])};
      }
      asm volatile("s_waitcnt lgkmcnt(0)" ::: "memory");
#pragma unroll
      for (int k = 0; k < 4; ++k) {
        const int r = 8 * k + (lane >> 3), f2_ = ((r & 3) << 2) | ((r >> 2) & 3);
        st16(rs, WS_BP + (r * 4 + (w >> 1)) * 32768 + u * 256 + (w & 1) * 128 + (lane & 7) * 16,
             LD128(r * 1024 + cb + ((((w & 1) * 8 + (lane & 7)) ^ f2_) << 4)));
      }
    }
    ST(8);
    vmwait<0>();
    bar();
    ST(9);
    v4u ml[2];
    if (w == 0) {
      if (lane < 32) {
        const float Ls = (LDF(L_LS + lane * 4) + LDF(L_LS + 128 + lane * 4)) + (LDF(L_LS + 256 + lane * 4) + LDF(L_LS + 384 + lane * 4));
        st16(rs, WS_ML + lane * 2048 + u * 16, v4u{fu(Mx), tag, fu(Ls), tag});
      }
      vmwait<0>();
      const int n = sweep<2>(rs, tag, ml, [&](int k) { return WS_ML + h * 2048 + k * 1024 + lane * 16; });
      STN(18, n);
    } else if (w >= 4) {
      const int jw = w - 4;
#pragma unroll
      for (int i = 0; i < 8; ++i) *(LAS v4u*)(L + L_WUV + (8 * jw + i) * 1152 + lane * 16) = tw[i];
#pragma unroll
      for (int i = 0; i < 9; ++i) *(LAS v4u*)(L + L_WOB + (9 * jw + i) * 1024 + lane * 16) = tw[8 + i];
    }
    bar();
    ST(10);
    v4u bp[4];
#pragma unroll
    for (int k = 0; k < 4; ++k) bp[k] = ld16(rs, WS_BP + u * 32768 + k * 8192 + tid * 16);
    asm volatile("" ::: "memory");
    if (w == 0) {
#pragma unroll
      for (int b = 0; b < 2; ++b) {
        const float m = uf(ml[b].x);
        float M = fmaxf(m, dpp<X1>(m)); M = fmaxf(M, dpp<X2>(M)); M = fmaxf(M, dpp<XH>(M)); M = fmaxf(M, dpp<X8>(M));
        M = sw32max(sw16max(M));
        const float f = ex2(m - M);
        float Ls = f * uf(ml[b].z);
        Ls += dpp<X1>(Ls); Ls += dpp<X2>(Ls); Ls += dpp<XH>(Ls); Ls += dpp<X8>(Ls);
        Ls = sw16add(Ls, Ls); Ls = sw32add(Ls, Ls);
        *(LAS float*)(L + L_F + b * 256 + lane * 4) = f * __builtin_amdgcn_rcpf(Ls);
      }
    }
    v4u uvf[4];
    {
      const int ub = L_WUV + 2 * g * 1152 + tq * 256 + (((2 * w + (tp >> 1)) ^ (tq << 1)) << 4) + (tp & 1) * 8;
#pragma unroll
      for (int s = 0; s < 4; ++s) uvf[s] = cat(TR(ub + s * 9216), TR(ub + s * 9216 + 1152));
      const int ob = L_WOB + (8 * g + tq) * 1152 + tp * 8;
#pragma unroll
      for (int n = 0; n < 4; ++n) wof[n] = cat(TR(ob + (4 * w + n) * 32), TR(ob + 4608 + (4 * w + n) * 32));
      wof[4] = cat(TR(ob + (32 + (w & 3)) * 32), TR(ob + 4608 + (32 + (w & 3)) * 32));
    }
    bar();
    {
      float acc[2][8];
#pragma unroll
      for (int b = 0; b < 2; ++b) {
        const float f0 = LDF(L_F + (64 * b + 4 * w + g) * 4), f1 = LDF(L_F + (64 * b + 32 + 4 * w + g) * 4);
#pragma unroll
        for (int e = 0; e < 4; ++e) {
          acc[b][2 * e] = fmaf(f1, lo16(bp[2 * b + 1][e]), f0 * lo16(bp[2 * b][e]));
          acc[b][2 * e + 1] = fmaf(f1, hi16(bp[2 * b + 1][e]), f0 * hi16(bp[2 * b][e]));
        }
      }
      ST(11);
      float t[8], r[4];
#pragma unroll
      for (int e = 0; e < 8; ++e) t[e] = sw32add(acc[0][e], acc[1][e]);
#pragma unroll
      for (int e = 0; e < 4; ++e) r[e] = sw16add(t[e], t[e + 4]);
      *(LAS v2u*)(L + L_CC + (2 * w + hh) * 272 + j16 * 16 + (g & 1) * 8) = v2u{pk(r[0], r[1]), pk(r[2], r[3])};
    }
    bar();
    {
      f4 d = {0.f, 0.f, 0.f, 0.f};
#pragma unroll
      for (int s = 0; s < 4; ++s) d = mfma32(LD128(L_CC + j16 * 272 + s * 64 + g * 16), uvf[s], d);
      float e = sw32add(d[0] + d[2], d[1] + d[3]);
      e = sw16add(e, e);
      if (!(g & 1)) st8(rs, WS_CX + u * 2048 + hh * 1024 + (16 * w + j16) * 8, v2u{fu(e), tag});
    }
    ST(12);
    if (w == 0) {
      vmwait<0>();
      v4u v[2];
      sweep<2>(rs, tag, v, [&](int k) { return WS_CX + (4 * h + g) * 2048 + k * 1024 + q4 * 256 + j16 * 16; });
      float e0 = sw32add(uf(v[0].x), uf(v[1].x)), e1 = sw32add(uf(v[0].z), uf(v[1].z));
      e0 = sw16add(e0, e0); e1 = sw16add(e1, e1);
      *(LAS unsigned*)(L + L_OV + hh * 64 + j16 * 4) = pk(e0, e1);
      ST(13);
    }
    bar();
  }
  {
    const v4u of = LD128(L_OV + (lane & 1) * 64 + g * 16);
    f4 d[5];
#pragma unroll
    for (int n = 0; n < 5; ++n) d[n] = mfma32(of, wof[n], f4{0.f, 0.f, 0.f, 0.f});
    const float y0 = (g & 2) ? ((g & 1) ? d[3][0] : d[2][0]) : ((g & 1) ? d[1][0] : d[0][0]);
    const float y1 = (g & 2) ? ((g & 1) ? d[3][1] : d[2][1]) : ((g & 1) ? d[1][1] : d[0][1]);
    const int ob = WS_OP + role * 72 * 16384 + u * 128 + (lane & 7) * 16;
    st16(rs, ob + (8 * w + (lane >> 3)) * 16384, v4u{fu(y0), tag, fu(y1), tag});
    if (w >= 4 && lane < 16) st16(rs, ob + (64 + 2 * (w & 3) + (lane >> 3)) * 16384, v4u{fu(d[4][0]), tag, fu(d[4][1]), tag});
  }
  if (role) ST(14); else ST(10);
  if (u < 72) {
    vmwait<0>();
    v4u v[2];
    const int n = sweep<2>(rs, tag, v, [&](int k) { return WS_OP + (role * 72 + u) * 16384 + w * 2048 + k * 1024 + lane * 16; });
    if (role) STN(19, n); else STN(18, n);
    if (!role) ST(11);
    float e = sw32add(uf(v[0].x) + uf(v[1].x), uf(v[0].z) + uf(v[1].z));
    e = sw16add(e, e);
    e += dpp<X8>(e);
    if ((lane & 24) == 0) *(LAS float*)(L + L_RED + w * 64 + (lane >> 5) * 32 + (lane & 7) * 4) = e;
    bar();
    if (tid < 16) {
      float s = 0.f;
#pragma unroll
      for (int k = 0; k < 8; ++k) s += LDF(L_RED + k * 64 + tid * 4);
      *(GAS unsigned short*)(a.out + ((tid >> 3) * 1152 + role * 576 + 8 * u + (tid & 7)) * 2) = (unsigned short)pk(s, s);
    }
  } else if (c == 255) {
    if (w < 4) {
      for (int n = 0; n < SPIN_CAP; ++n) {
        asm volatile("" ::: "memory");
        const v2u v = ld8(rs, WS_QP + tid * 2048);
        if (__builtin_amdgcn_ballot_w64(v.y != tag) == 0) break;
        __builtin_amdgcn_s_sleep(2);
      }
    }
    bar();
    if (tid == 0) __builtin_amdgcn_raw_buffer_store_b32(tag, rs, WS_E, 0, 16);
  }
  if (role) ST(15); else ST(12);
  if (role == PROBE_ROLE && stp[PROBE_K]) {
    const unsigned tend = stp[role ? 15 : 12] + PROBE_MULT * (stp[PROBE_K] - stp[PROBE_K0]);
    for (;;) { unsigned long long t_; asm volatile("s_memrealtime %0\n\ts_waitcnt lgkmcnt(0)" : "=s"(t_) :: "memory"); if ((int)((unsigned)t_ - tend) >= 0) break; __builtin_amdgcn_s_sleep(4); }
  }
  if (lane == 0) {
#pragma unroll
    for (int k = 0; k < NSTAMP; ++k) ((GAS unsigned*)(a.ws + STAMP_OFF))[(c * 8 + w) * NSTAMP + k] = stp[k];
  }
}

extern "C" void kernel_launch(void* const* d_in, const int* in_sizes, int n_in, void* d_out, int out_size, void* d_ws, size_t ws_size, hipStream_t stream) {
  Args a;
  a.x[0] = (const char*)d_in[0]; a.x[1] = (const char*)d_in[5];
  a.wq[0] = (const char*)d_in[3]; a.wq[1] = (const char*)d_in[7];
  a.wo[0] = (const char*)d_in[4]; a.wo[1] = (const char*)d_in[10];
  a.kc = (const char*)d_in[1]; a.vc = (const char*)d_in[2]; a.ckv = (const char*)d_in[6];
  a.wuk = (const char*)d_in[8]; a.wuv = (const char*)d_in[9];
  a.out = (char*)d_out; a.ws = (char*)d_ws;
  fused_decode<<<256, 512, 0, stream>>>(a);
}
```

```cpp
#define XF
#include <hip/hip_runtime.h>
#include <stdint.h>

typedef unsigned v4u __attribute__((ext_vector_type(4)));
typedef unsigned v2u __attribute__((ext_vector_type(2)));
typedef float f2 __attribute__((ext_vector_type(2)));
typedef float f4 __attribute__((ext_vector_type(4)));
typedef float f16v __attribute__((ext_vector_type(16)));
typedef short s4 __attribute__((ext_vector_type(4)));
typedef __bf16 b2 __attribute__((ext_vector_type(2)));
typedef __bf16 b8 __attribute__((ext_vector_type(8)));
#define LAS __attribute__((address_space(3)))
#define GAS __attribute__((address_space(1)))
#define DI __device__ __forceinline__

#define A_W0 2
#define A_NSLOW 3
#define A_W 4
#define B_W 6

#define CODE_KB 15
constexpr float C2 = 0.08838834764831845f * 1.4426950408889634f;
constexpr int SPIN_CAP = 1 << 16;
constexpr int WS_E = 0, WS_QP = 4096, WS_QA = WS_QP + 524288, WS_ML = WS_QA + 131072, WS_CX = WS_ML + 65536,
              WS_AP = WS_CX + 262144, WS_AM = WS_AP + 262144, WS_OP = WS_AM + 16384, WS_BP = WS_OP + 2359296,
              WS_END = WS_BP + 4194304;
constexpr int L_WA = 110592, L_WQB = 89600, L_QIMG = 131072, L_SCR = 152064;
constexpr int L_X = L_SCR, L_QV = L_SCR + 640, L_FLAG = L_SCR + 1152, L_DUMMY = L_SCR + 1280, L_MRG = L_SCR + 2304,
              L_MLS = L_SCR + 10496, L_OV = L_SCR + 10624, L_RED = L_SCR + 10752;
constexpr int L_MX = L_QIMG, L_LS = L_QIMG + 512, L_PF = L_QIMG + 1024, L_F = L_QIMG + 12288, L_CC = L_QIMG + 13312;
constexpr int L_WUV = 0, L_WOB = 36864;

#define ST(k) do {} while (0)
#define STN(k, n) do { (void)(n); } while (0)

struct Args {
  const char* x[2]; const char* wq[2]; const char* wo[2];
  const char* kc; const char* vc; const char* ckv; const char* wuk; const char* wuv;
  char* out; char* ws;
};

template <int N> DI void vmwait() { asm volatile("s_waitcnt vmcnt(%0) lgkmcnt(0)" ::"n"(N) : "memory"); }
DI void bar() { asm volatile("s_waitcnt lgkmcnt(0)\n\ts_barrier" ::: "memory"); }
DI void dma_nt(unsigned lds, unsigned voff, const char* base) {
  unsigned keep;
  asm volatile("s_mov_b32 %0, m0\n\ts_mov_b32 m0, %1\n\ts_nop 2\n\tglobal_load_lds_dwordx4 %2, %3 nt\n\ts_mov_b32 m0, %0"
               : "=&s"(keep) : "s"(lds), "v"(voff), "s"(base) : "memory");
}
DI void dma_def(unsigned lds, unsigned voff, const char* base) {
  unsigned keep;
  asm volatile("s_mov_b32 %0, m0\n\ts_mov_b32 m0, %1\n\ts_nop 2\n\tglobal_load_lds_dwordx4 %2, %3\n\ts_mov_b32 m0, %0"
               : "=&s"(keep) : "s"(lds), "v"(voff), "s"(base) : "memory");
}
typedef __amdgpu_buffer_rsrc_t rsrc_t;
DI v4u ld16(rsrc_t rs, int off) { return __builtin_amdgcn_raw_buffer_load_b128(rs, off, 0, 16); }
DI v2u ld8(rsrc_t rs, int off) { return __builtin_amdgcn_raw_buffer_load_b64(rs, off, 0, 16); }
DI void st16(rsrc_t rs, int off, v4u v) { __builtin_amdgcn_raw_buffer_store_b128(v, rs, off, 0, 16); }
DI void st8(rsrc_t rs, int off, v2u v) { __builtin_amdgcn_raw_buffer_store_b64(v, rs, off, 0, 16); }
DI unsigned fu(float x) { return __float_as_uint(x); }
DI float uf(unsigned x) { return __uint_as_float(x); }
DI unsigned pk(float lo, float hi) { return __builtin_bit_cast(unsigned, __builtin_convertvector(f2{lo, hi}, b2)); }
DI float lo16(unsigned x) { return uf(x << 16); }
DI float hi16(unsigned x) { return uf(x & 0xffff0000u); }
template <int CTRL> DI float dpp(float x) { return uf(__builtin_amdgcn_mov_dpp(fu(x), CTRL, 0xf, 0xf, true)); }
constexpr int X1 = 0xB1, X2 = 0x4E, XH = 0x141, X8 = 0x128;
DI float sw32add(float a, float b) { auto r = __builtin_amdgcn_permlane32_swap(fu(a), fu(b), false, false); return uf(r[0]) + uf(r[1]); }
DI float sw16add(float a, float b) { auto r = __builtin_amdgcn_permlane16_swap(fu(a), fu(b), false, false); return uf(r[0]) + uf(r[1]); }
DI float sw32max(float a) { auto r = __builtin_amdgcn_permlane32_swap(fu(a), fu(a), false, false); return fmaxf(uf(r[0]), uf(r[1])); }
DI float sw16max(float a) { auto r = __builtin_amdgcn_permlane16_swap(fu(a), fu(a), false, false); return fmaxf(uf(r[0]), uf(r[1])); }
DI f4 mfma32(v4u a, v4u b, f4 c) { return __builtin_amdgcn_mfma_f32_16x16x32_bf16(__builtin_bit_cast(b8, a), __builtin_bit_cast(b8, b), c, 0, 0, 0); }
DI f4 mfma16(v2u a, v2u b, f4 c) { return __builtin_amdgcn_mfma_f32_16x16x16bf16_1k(__builtin_bit_cast(s4, a), __builtin_bit_cast(s4, b), c, 0, 0, 0); }
DI f16v mfmaB(v4u a, v4u b, f16v c) { return __builtin_amdgcn_mfma_f32_32x32x16_bf16(__builtin_bit_cast(b8, a), __builtin_bit_cast(b8, b), c, 0, 0, 0); }
DI v4u cat(v2u a, v2u b) { return v4u{a.x, a.y, b.x, b.y}; }
DI float ex2(float x) { return __builtin_amdgcn_exp2f(x); }

template <int N, class F> DI int sweep(rsrc_t rs, unsigned tag, v4u (&v)[N], F off) {
  int n = 0;
  for (;;) {
    asm volatile("" ::: "memory");
#pragma unroll
    for (int k = 0; k < N; ++k) v[k] = ld16(rs, off(k));
    unsigned bad = 0;
#pragma unroll
    for (int k = 0; k < N; ++k) bad |= (v[k].y ^ tag) | (v[k].w ^ tag);
    ++n;
    if (__builtin_amdgcn_ballot_w64(bad != 0) == 0 || n > SPIN_CAP) break;
    __builtin_amdgcn_s_sleep(2);
  }
  return n;
}

__global__ void __launch_bounds__(512, 2) fused_decode(Args a) {
  __shared__ __attribute__((aligned(16))) char smem[163840];
  LAS char* const L = (LAS char*)smem;
  const unsigned LB = (unsigned)(uintptr_t)L;
#define LD128(o) (*(const LAS v4u*)(L + (o)))
#define LD64(o) (*(const LAS v2u*)(L + (o)))
#define LDF(o) (*(const LAS float*)(L + (o)))
#define TR(o) __builtin_bit_cast(v2u, __builtin_amdgcn_ds_read_tr16_b64_v4i16((LAS s4*)(L + (o))))
  ST(0);
  const int c = blockIdx.x, tid = threadIdx.x, lane = tid & 63;
  const int w = __builtin_amdgcn_readfirstlane(tid >> 6);
  const int role = (c >> 3) & 1, u = (c & 7) * 16 + (c >> 4), h = u >> 2, q4 = u & 3;
  const int g = lane >> 4, j16 = lane & 15, tq = (lane >> 2) & 3, tp = lane & 3;

  unsigned ev;
  { const unsigned z = 0; asm volatile("s_nop 4\n\tglobal_load_dword %0, %1, %2 sc1" : "=v"(ev) : "v"(z), "s"(a.ws) : "memory"); }
  const unsigned voffV = g * 256 + ((j16 ^ (g << 1)) << 4);
  const int lwq = role ? L_WQB : L_WA;
  {
    const char* wq = a.wq[role] + (size_t)(144 * q4) * 8192 + h * 256;
    const unsigned vo = g * 8192 + ((j16 ^ (g << 1)) << 4);
    if (w == 0) { if (lane < 36) dma_def(LB + L_X, (lane >= 18 ? 1152 - 288 : 0) + lane * 16, a.x[role] + 288 * q4); }
    if (w == 1) {
      unsigned long long pc; asm volatile("s_getpc_b64 %0" : "=s"(pc));
      const int ch = c >> 3;
      if (ch < CODE_KB) dma_def(LB + L_DUMMY, lane * 16, (const char*)(pc & ~255ull) + ch * 1024);
    }
    if (tid == 128) *(LAS unsigned*)(L + L_FLAG) = 0u;
#pragma unroll
    for (int i = 0; i < 4; ++i) {
      if (i >= A_W0 && !role) vmwait<A_W0 - 1>();
      dma_nt(LB + lwq + (w + 8 * i) * 1152, vo, wq + (size_t)(w + 8 * i) * 32768);
    }
    if (4 >= A_W0 && !role) vmwait<A_W0 - 1>();
    const int rl = 2 * (w & 1) + g;
    if (lane < 32) dma_nt(LB + lwq + (32 + (w >> 1)) * 1152 + (w & 1) * 512, rl * 8192 + ((j16 ^ (rl << 1)) << 4), wq + (size_t)(32 + (w >> 1)) * 32768);
  }
  const rsrc_t rs = __builtin_amdgcn_make_buffer_rsrc(a.ws, 0, WS_END, 0x00020000);
  v4u wuk[8];
  if (role && w < 4) {
    const rsrc_t ru = __builtin_amdgcn_make_buffer_rsrc((void*)a.wuk, 0, 4194304, 0x00020000);
    const int o = (128 * q4 + 32 * w + 4 * tq) * 8192 + h * 256 + (4 * tp + g) * 16;
#pragma unroll
    for (int i = 0; i < 8; ++i) wuk[i] = __builtin_amdgcn_raw_buffer_load_b128(ru, o, ((i >> 2) * 16 + (i & 3)) * 8192, 2);
    vmwait<8>();
  } else {
    vmwait<0>();
  }
  asm volatile("" : "+v"(ev));
  const unsigned tag = __builtin_amdgcn_readfirstlane(ev) + 1u;
  bar();
  ST(1);
  {
    const int tb = lwq + g * 1152 + tq * 256 + (((2 * w + (tp >> 1)) ^ (tq << 1)) << 4) + (tp & 1) * 8;
    const int xb = L_X + (lane & 1) * 288 + g * 8;
    f4 acc = {0.f, 0.f, 0.f, 0.f};
#pragma unroll
    for (int t = 0; t < 8; t += 2)
      acc = mfma32(cat(LD64(xb + t * 32), LD64(xb + t * 32 + 32)), cat(TR(tb + t * 4608), TR(tb + t * 4608 + 4608)), acc);
    acc += mfma16(LD64(xb + 256), TR(tb + 8 * 4608), f4{0.f, 0.f, 0.f, 0.f});
    if (lane < 32) st8(rs, WS_QP + (role * 128 + u) * 2048 + g * 1024 + (16 * w + j16) * 8, v2u{fu(g ? acc[1] : acc[0]), tag});
  }
  bar();
  ST(2);
  if (w == 0) {
    vmwait<0>();
    v4u v[8];
    const int base = WS_QP + lane * 16 + (role ? (128 + 4 * h) * 2048 : (u & ~7) * 2048 + ((u >> 2) & 1) * 1024);
    const int vs = role ? 1024 : 8192;
    const int n = sweep<8>(rs, tag, v, [&](int k) { return base + (k >> 2) * vs + (k & 3) * 2048; });
    STN(16, n);
#pragma unroll
    for (int k = 0; k < 2; ++k)
      *(LAS unsigned*)(L + L_QV + k * 256 + lane * 4) =
          pk((uf(v[4 * k].x) + uf(v[4 * k + 1].x)) + (uf(v[4 * k + 2].x) + uf(v[4 * k + 3].x)),
             (uf(v[4 * k].z) + uf(v[4 * k + 1].z)) + (uf(v[4 * k + 2].z) + uf(v[4 * k + 3].z)));
    asm volatile("s_waitcnt lgkmcnt(0)" ::: "memory");
    *(volatile LAS unsigned*)(L + L_FLAG) = 1u;
    ST(3);
  }
  v4u wof[5];
  if (!role) {
    const size_t kvo = ((size_t)(((u >> 2) & 1) * 16 + (u >> 3)) * 8192 + 6144 + 512 * q4 + 16 * w) * 256;
    const char* kp = a.kc + kvo;
    const char* vp = a.vc + kvo;
    const char* wo = a.wo[0] + (size_t)u * 36864 + w * 4608;
    const unsigned voffK = g * 256 + ((j16 ^ g) << 4);
    const int ring = w * 13824;
    const int kb = ring + tq * 1088 + tp * 256 + ((g ^ tp) << 4);
    const int vb = ring + g * 1152 + tq * 256 + (tp & 1) * 8;
    v4u qf[4];
    f4 sc[4], oc[8];
    v2u pf[4];
    float mw = 0.f, lw = 0.f;
#pragma unroll
    for (int n = 0; n < 8; ++n) oc[n] = f4{0.f, 0.f, 0.f, 0.f};
#pragma unroll
    for (int n = 0; n <= 31 + A_W; ++n) {
      if (n < A_NSLOW) vmwait<A_W0 - 1>(); else if (n < 37) vmwait<A_W - 1>(); else vmwait<5>();
#pragma unroll
      for (int m = n; m < (n == 31 + A_W ? 37 : n + 1); ++m) {
        const int T = m >> 2, i = m & 3;
        if (m < 16) dma_nt(LB + ring + (T % 3) * 4608 + i * 1088, voffK, kp + T * 32768 + i * 1024);
        else if (m < 32) dma_nt(LB + ring + (T % 3) * 4608 + i * 1152, voffV, vp + (T - 4) * 32768 + i * 1024);
        else if (m < 36) dma_nt(LB + L_WA + w * 4608 + i * 1024, lane * 16, wo + i * 1024);
        else if (m == 36 && lane < 32) dma_nt(LB + L_WA + w * 4608 + 4096, lane * 16, wo + 4096);
      }
      const int d = n - 3 - A_W;
      if (d >= 0 && (d & 3) == 0) {
        const int T = d >> 2;
        if (T == 0) {
          while (*(volatile LAS unsigned*)(L + L_FLAG) == 0u) __builtin_amdgcn_s_sleep(1);
          asm volatile("" ::: "memory");
#pragma unroll
          for (int s = 0; s < 4; ++s) qf[s] = LD128(L_QV + (lane & 1) * 256 + s * 64 + g * 16);
          ST(4);
        }
        if (T < 4) {
          f4 s4_ = {0.f, 0.f, 0.f, 0.f};
#pragma unroll
          for (int s = 0; s < 4; ++s) s4_ = mfma32(LD128(kb + (T % 3) * 4608 + s * 64), qf[s], s4_);
          sc[T] = s4_;
          if (T == 3) {
            float m = -INFINITY;
#pragma unroll
            for (int t = 0; t < 4; ++t) m = fmaxf(fmaxf(fmaxf(sc[t][0], sc[t][1]), fmaxf(sc[t][2], sc[t][3])), m);
            m = sw32max(sw16max(m));
            mw = m * C2;
            float l = 0.f;
#pragma unroll
            for (int t = 0; t < 4; ++t) {
              const float p0 = ex2(fmaf(sc[t][0], C2, -mw)), p1 = ex2(fmaf(sc[t][1], C2, -mw)), p2 = ex2(fmaf(sc[t][2], C2, -mw)), p3 = ex2(fmaf(sc[t][3], C2, -mw));
              l += (p0 + p1) + (p2 + p3);
              pf[t] = v2u{pk(p0, p1), pk(p2, p3)};
            }
            l = sw16add(l, l);
            lw = sw32add(l, l);
            ST(5);
          }
        } else {
          if (T == 7) ST(6);
#pragma unroll
          for (int n2 = 0; n2 < 8; ++n2)
            oc[n2] = mfma16(pf[T - 4], TR(vb + (T % 3) * 4608 + (((2 * n2 + (tp >> 1)) ^ (tq << 1)) << 4)), oc[n2]);
        }
      }
    }
    if (lane < 16) {
#pragma unroll
      for (int n = 0; n < 8; ++n) *(LAS f2*)(L + L_MRG + w * 1024 + (16 * n + lane) * 8) = f2{oc[n][0], oc[n][1]};
      if (lane < 2) *(LAS f2*)(L + L_MLS + w * 16 + lane * 8) = f2{mw, lw};
    }
    ST(7);
    bar();
    if (w < 4) {
      const int hh = w >> 1, d = tid & 127;
      f2 ml[8];
      float M = -INFINITY;
#pragma unroll
      for (int k = 0; k < 8; ++k) { ml[k] = *(const LAS f2*)(L + L_MLS + k * 16 + hh * 8); M = fmaxf(M, ml[k].x); }
      float o = 0.f, Ls = 0.f;
#pragma unroll
      for (int k = 0; k < 8; ++k) { const float f = ex2(ml[k].x - M); o = fmaf(f, LDF(L_MRG + k * 1024 + d * 8 + hh * 4), o); Ls = fmaf(f, ml[k].y, Ls); }
      st8(rs, WS_AP + u * 2048 + tid * 8, v2u{fu(o), tag});
      if (d == 0) st16(rs, WS_AM + u * 128 + hh * 16, v4u{fu(M), tag, fu(Ls), tag});
    }
    ST(8);
    vmwait<0>();
    if (w == 0) {
      v4u v[4];
      const int p0 = (u & ~7) + g, hh = h & 1;
      const int n = sweep<4>(rs, tag, v, [&](int k) {
        return k < 2 ? WS_AP + (p0 + 4 * k) * 2048 + hh * 1024 + q4 * 256 + j16 * 16 : WS_AM + (p0 + 4 * (k - 2)) * 128 + hh * 16; });
      STN(17, n);
      float r[2][2];
#pragma unroll
      for (int b = 0; b < 2; ++b) {
        const float m = uf(v[2 + b].x), M = sw32max(sw16max(m)), f = ex2(m - M);
        float Ls = f * uf(v[2 + b].z);
        Ls = sw16add(Ls, Ls); Ls = sw32add(Ls, Ls);
        const float s = f * __builtin_amdgcn_rcpf(Ls);
        r[b][0] = s * uf(v[b].x); r[b][1] = s * uf(v[b].z);
      }
      float e0 = sw32add(r[0][0], r[1][0]), e1 = sw32add(r[0][1], r[1][1]);
      e0 = sw16add(e0, e0); e1 = sw16add(e1, e1);
      *(LAS unsigned*)(L + L_OV + (lane >> 5) * 64 + j16 * 4) = pk(e0, e1);
      ST(9);
    }
    bar();
    {
      const int ob = L_WA + (8 * g + tq) * 1152 + tp * 8;
#pragma unroll
      for (int n = 0; n < 4; ++n) wof[n] = cat(TR(ob + (4 * w + n) * 32), TR(ob + 4608 + (4 * w + n) * 32));
      wof[4] = cat(TR(ob + (32 + (w & 3)) * 32), TR(ob + 4608 + (32 + (w & 3)) * 32));
    }
  } else {
    const int bb = u >> 6;
    v4u qa[16];
    if (w < 4) {
      while (*(volatile LAS unsigned*)(L + L_FLAG) == 0u) {}
      asm volatile("" ::: "memory");
      const v4u qf = LD128(L_QV + (tq & 1) * 256 + tp * 64 + g * 16);
      float r2[2];
#pragma unroll
      for (int gr = 0; gr < 2; ++gr) {
        float s[4];
#pragma unroll
        for (int i = 0; i < 4; ++i) {
          const f4 d = mfma32(wuk[4 * gr + i], qf, f4{0.f, 0.f, 0.f, 0.f});
          s[i] = (tp & 2) ? ((tp & 1) ? d[3] : d[2]) : ((tp & 1) ? d[1] : d[0]);
        }
        const float a0 = ((tp & 1) ? s[1] : s[0]) + dpp<X1>((tp & 1) ? s[0] : s[1]);
        const float a1 = ((tp & 1) ? s[3] : s[2]) + dpp<X1>((tp & 1) ? s[2] : s[3]);
        r2[gr] = ((tp & 2) ? a1 : a0) + dpp<X2>((tp & 2) ? a0 : a1);
      }
      const float r = ((tq & 2) ? r2[1] : r2[0]) * C2;
      const float rn = dpp<X1>(r);
      if (!(tp & 1))
        st8(rs, WS_QA + (tq & 1) * 65536 + (8 * q4 + 2 * w + (tq >> 1)) * 2048 + h * 64 + ((g >> 1) ^ ((h >> 3) & 1)) * 32 + (g & 1) * 16 + (tp >> 1) * 8,
            v2u{pk(r, rn), tag});
      ST(4);
      vmwait<0>();
      const int base = WS_QA + bb * 65536 + w * 16384 + lane * 16;
      const int n = sweep<16>(rs, tag, qa, [&](int k) { return base + k * 1024; });
      STN(17, n);
    } else {
      const int jw = w - 4;
      const char* cp = a.ckv + (size_t)u * 131072 + jw * 1024;
#pragma unroll 4
      for (int i = 0; i < 32; ++i) {
        vmwait<B_W - 1>();
        dma_nt(LB + (4 * i + jw) * 1024, (lane ^ ((jw << 2) | (i & 3))) << 4, cp + i * 4096);
      }
    }
    ST(5);
    bar();
    if (w < 4) {
#pragma unroll
      for (int k = 0; k < 16; ++k) *(LAS v2u*)(L + L_QIMG + w * 8192 + k * 512 + lane * 8) = v2u{qa[k].x, qa[k].z};
    }
    vmwait<0>();
    bar();
    ST(6);
    const int hd = lane & 31, hh = lane >> 5;
    v4u tw[17];
    f16v X;
    float mloc = 0.f;
    if (w < 4) {
      const int fr = ((hd & 3) << 2) | ((hd >> 2) & 3);
      const int ab = (32 * w + hd) * 1024, qb = L_QIMG + hd * 32 + (hh ^ ((hd >> 3) & 1)) * 16;
#pragma unroll
      for (int r = 0; r < 16; ++r) X[r] = 0.f;
#pragma unroll
      for (int s = 0; s < 32; ++s)
        X = mfmaB(LD128(ab + (s >> 3) * 256 + (((2 * (s & 7) + hh) ^ fr) << 4)), LD128(qb + s * 1024), X);
      float m = X[0];
#pragma unroll
      for (int r = 1; r < 16; ++r) m = fmaxf(m, X[r]);
      mloc = sw32max(m);
    } else {
      const int jw = w - 4;
      const rsrc_t rv = __builtin_amdgcn_make_buffer_rsrc((void*)a.wuv, 0, 4194304, 0x00020000);
      const rsrc_t ro = __builtin_amdgcn_make_buffer_rsrc((void*)a.wo[1], 0, 4718592, 0x00020000);
      const int o = (128 * q4 + (jw >> 1) * 64 + (jw & 1) * 4 + g) * 8192 + h * 256 + ((j16 ^ (g << 1)) << 4);
#pragma unroll
      for (int i = 0; i < 8; ++i) tw[i] = __builtin_amdgcn_raw_buffer_load_b128(rv, o, i * 8 * 8192, 2);
#pragma unroll
      for (int i = 0; i < 9; ++i) tw[8 + i] = __builtin_amdgcn_raw_buffer_load_b128(ro, u * 36864 + jw * 9216 + lane * 16, i * 1024, 2);
    }
    bar();
    if (w < 4 && lane < 32) *(LAS float*)(L + L_MX + w * 128 + lane * 4) = mloc;
    bar();
    float Mx = 0.f;
    if (w < 4) {
      Mx = fmaxf(fmaxf(LDF(L_MX + hd * 4), LDF(L_MX + 128 + hd * 4)), fmaxf(LDF(L_MX + 256 + hd * 4), LDF(L_MX + 384 + hd * 4)));
      float l = 0.f;
#pragma unroll
      for (int r = 0; r < 16; ++r) { X[r] = ex2(X[r] - Mx); l += X[r]; }
#pragma unroll
      for (int s = 0; s < 2; ++s)
        *(LAS v4u*)(L + L_PF + (2 * w + s) * 1024 + lane * 16) =
            v4u{pk(X[8 * s], X[8 * s + 1]), pk(X[8 * s + 2], X[8 * s + 3]), pk(X[8 * s + 4], X[8 * s + 5]), pk(X[8 * s + 6], X[8 * s + 7])};
      l = sw32add(l, l);
      if (lane < 32) *(LAS float*)(L + L_LS + w * 128 + lane * 4) = l;
    }
    bar();
    ST(7);
    {
      f16v O[2];
#pragma unroll
      for (int r = 0; r < 16; ++r) { O[0][r] = 0.f; O[1][r] = 0.f; }
      const int cb = (w >> 1) * 256, c0 = (w & 1) * 8 + 2 * (g & 1) + (tp >> 1);
      int tb[2][2];
#pragma unroll
      for (int ct = 0; ct < 2; ++ct)
#pragma unroll
        for (int tt = 0; tt < 2; ++tt) tb[ct][tt] = (8 * tt + 4 * hh + tq) * 1024 + cb + (((c0 + 4 * ct) ^ ((tq << 2) | (2 * tt + hh))) << 4) + (tp & 1) * 8;
#pragma unroll
      for (int ks = 0; ks < 8; ++ks) {
        const v4u p = LD128(L_PF + ks * 1024 + lane * 16);
#pragma unroll
        for (int ct = 0; ct < 2; ++ct) O[ct] = mfmaB(cat(TR(tb[ct][0] + ks * 16384), TR(tb[ct][1] + ks * 16384)), p, O[ct]);
      }
      const int fr = ((hd & 3) << 2) | ((hd >> 2) & 3);
#pragma unroll
      for (int n = 0; n < 4; ++n) {
        const f16v& o = O[n >> 1];
        const int r = (n & 1) * 8;
        *(LAS v4u*)(L + hd * 1024 + cb + ((((w & 1) * 8 + 4 * hh + n) ^ fr) << 4)) =
            v4u{pk(o[r], o[r + 1]), pk(o[r + 2], o[r + 3]), pk(o[r + 4], o[r + 5]), pk(o[r + 6], o[r + 7])};
      }
      asm volatile("s_waitcnt lgkmcnt(0)" ::: "memory");
#pragma unroll
      for (int k = 0; k < 4; ++k) {
        const int r = 8 * k + (lane >> 3), f2_ = ((r & 3) << 2) | ((r >> 2) & 3);
        st16(rs, WS_BP + (r * 4 + (w >> 1)) * 32768 + u * 256 + (w & 1) * 128 + (lane & 7) * 16,
             LD128(r * 1024 + cb + ((((w & 1) * 8 + (lane & 7)) ^ f2_) << 4)));
      }
    }
    ST(8);
    vmwait<0>();
    bar();
    ST(9);
    v4u ml[2];
    if (w == 0) {
      if (lane < 32) {
        const float Ls = (LDF(L_LS + lane * 4) + LDF(L_LS + 128 + lane * 4)) + (LDF(L_LS + 256 + lane * 4) + LDF(L_LS + 384 + lane * 4));
        st16(rs, WS_ML + lane * 2048 + u * 16, v4u{fu(Mx), tag, fu(Ls), tag});
      }
      vmwait<0>();
      const int n = sweep<2>(rs, tag, ml, [&](int k) { return WS_ML + h * 2048 + k * 1024 + lane * 16; });
      STN(18, n);
    } else if (w >= 4) {
      const int jw = w - 4;
#pragma unroll
      for (int i = 0; i < 8; ++i) *(LAS v4u*)(L + L_WUV + (8 * jw + i) * 1152 + lane * 16) = tw[i];
#pragma unroll
      for (int i = 0; i < 9; ++i) *(LAS v4u*)(L + L_WOB + (9 * jw + i) * 1024 + lane * 16) = tw[8 + i];
    }
    bar();
    ST(10);
    v4u bp[4];
#pragma unroll
    for (int k = 0; k < 4; ++k) bp[k] = ld16(rs, WS_BP + u * 32768 + k * 8192 + tid * 16);
    asm volatile("" ::: "memory");
    if (w == 0) {
#pragma unroll
      for (int b = 0; b < 2; ++b) {
        const float m = uf(ml[b].x);
        float M = fmaxf(m, dpp<X1>(m)); M = fmaxf(M, dpp<X2>(M)); M = fmaxf(M, dpp<XH>(M)); M = fmaxf(M, dpp<X8>(M));
        M = sw32max(sw16max(M));
        const float f = ex2(m - M);
        float Ls = f * uf(ml[b].z);
        Ls += dpp<X1>(Ls); Ls += dpp<X2>(Ls); Ls += dpp<XH>(Ls); Ls += dpp<X8>(Ls);
        Ls = sw16add(Ls, Ls); Ls = sw32add(Ls, Ls);
        *(LAS float*)(L + L_F + b * 256 + lane * 4) = f * __builtin_amdgcn_rcpf(Ls);
      }
    }
    v4u uvf[4];
    {
      const int ub = L_WUV + 2 * g * 1152 + tq * 256 + (((2 * w + (tp >> 1)) ^ (tq << 1)) << 4) + (tp & 1) * 8;
#pragma unroll
      for (int s = 0; s < 4; ++s) uvf[s] = cat(TR(ub + s * 9216), TR(ub + s * 9216 + 1152));
      const int ob = L_WOB + (8 * g + tq) * 1152 + tp * 8;
#pragma unroll
      for (int n = 0; n < 4; ++n) wof[n] = cat(TR(ob + (4 * w + n) * 32), TR(ob + 4608 + (4 * w + n) * 32));
      wof[4] = cat(TR(ob + (32 + (w & 3)) * 32), TR(ob + 4608 + (32 + (w & 3)) * 32));
    }
    bar();
    {
      float acc[2][8];
#pragma unroll
      for (int b = 0; b < 2; ++b) {
        const float f0 = LDF(L_F + (64 * b + 4 * w + g) * 4), f1 = LDF(L_F + (64 * b + 32 + 4 * w + g) * 4);
#pragma unroll
        for (int e = 0; e < 4; ++e) {
          acc[b][2 * e] = fmaf(f1, lo16(bp[2 * b + 1][e]), f0 * lo16(bp[2 * b][e]));
          acc[b][2 * e + 1] = fmaf(f1, hi16(bp[2 * b + 1][e]), f0 * hi16(bp[2 * b][e]));
        }
      }
      ST(11);
      float t[8], r[4];
#pragma unroll
      for (int e = 0; e < 8; ++e) t[e] = sw32add(acc[0][e], acc[1][e]);
#pragma unroll
      for (int e = 0; e < 4; ++e) r[e] = sw16add(t[e], t[e + 4]);
      *(LAS v2u*)(L + L_CC + (2 * w + hh) * 272 + j16 * 16 + (g & 1) * 8) = v2u{pk(r[0], r[1]), pk(r[2], r[3])};
    }
    bar();
    {
      f4 d = {0.f, 0.f, 0.f, 0.f};
#pragma unroll
      for (int s = 0; s < 4; ++s) d = mfma32(LD128(L_CC + j16 * 272 + s * 64 + g * 16), uvf[s], d);
      float e = sw32add(d[0] + d[2], d[1] + d[3]);
      e = sw16add(e, e);
      if (!(g & 1)) st8(rs, WS_CX + u * 2048 + hh * 1024 + (16 * w + j16) * 8, v2u{fu(e), tag});
    }
    ST(12);
    if (w == 0) {
      vmwait<0>();
      v4u v[2];
      sweep<2>(rs, tag, v, [&](int k) { return WS_CX + (4 * h + g) * 2048 + k * 1024 + q4 * 256 + j16 * 16; });
      float e0 = sw32add(uf(v[0].x), uf(v[1].x)), e1 = sw32add(uf(v[0].z), uf(v[1].z));
      e0 = sw16add(e0, e0); e1 = sw16add(e1, e1);
      *(LAS unsigned*)(L + L_OV + hh * 64 + j16 * 4) = pk(e0, e1);
      ST(13);
    }
    bar();
  }
  {
    const v4u of = LD128(L_OV + (lane & 1) * 64 + g * 16);
    f4 d[5];
#pragma unroll
    for (int n = 0; n < 5; ++n) d[n] = mfma32(of, wof[n], f4{0.f, 0.f, 0.f, 0.f});
    const float y0 = (g & 2) ? ((g & 1) ? d[3][0] : d[2][0]) : ((g & 1) ? d[1][0] : d[0][0]);
    const float y1 = (g & 2) ? ((g & 1) ? d[3][1] : d[2][1]) : ((g & 1) ? d[1][1] : d[0][1]);
    const int ob = WS_OP + role * 72 * 16384 + u * 128 + (lane & 7) * 16;
    st16(rs, ob + (8 * w + (lane >> 3)) * 16384, v4u{fu(y0), tag, fu(y1), tag});
    if (w >= 4 && lane < 16) st16(rs, ob + (64 + 2 * (w & 3) + (lane >> 3)) * 16384, v4u{fu(d[4][0]), tag, fu(d[4][1]), tag});
  }
  if (role) ST(14); else ST(10);
  if (u < 72) {
    vmwait<0>();
    v4u v[2];
    const int n = sweep<2>(rs, tag, v, [&](int k) { return WS_OP + (role * 72 + u) * 16384 + w * 2048 + k * 1024 + lane * 16; });
    if (role) STN(19, n); else STN(18, n);
    if (!role) ST(11);
    float e = sw32add(uf(v[0].x) + uf(v[1].x), uf(v[0].z) + uf(v[1].z));
    e = sw16add(e, e);
    e += dpp<X8>(e);
    if ((lane & 24) == 0) *(LAS float*)(L + L_RED + w * 64 + (lane >> 5) * 32 + (lane & 7) * 4) = e;
    bar();
    if (tid < 16) {
      float s = 0.f;
#pragma unroll
      for (int k = 0; k < 8; ++k) s += LDF(L_RED + k * 64 + tid * 4);
      *(GAS unsigned short*)(a.out + ((tid >> 3) * 1152 + role * 576 + 8 * u + (tid & 7)) * 2) = (unsigned short)pk(s, s);
    }
  } else if (c == 255) {
    if (w < 4) {
      for (int n = 0; n < SPIN_CAP; ++n) {
        asm volatile("" ::: "memory");
        const v2u v = ld8(rs, WS_QP + tid * 2048);
        if (__builtin_amdgcn_ballot_w64(v.y != tag) == 0) break;
        __builtin_amdgcn_s_sleep(2);
      }
    }
    bar();
    if (tid == 0) __builtin_amdgcn_raw_buffer_store_b32(tag, rs, WS_E, 0, 16);
  }
  if (role) ST(15); else ST(12);
}

__global__ void k_stamp(char* ws) { if (threadIdx.x == 0) *(GAS unsigned long long*)(ws + (20 << 20)) = __builtin_amdgcn_s_memrealtime(); }
__global__ void k_spin(char* ws) { const unsigned long long t0 = *(const GAS unsigned long long*)(ws + (20 << 20)); while (__builtin_amdgcn_s_memrealtime() < t0 + 10000) __builtin_amdgcn_s_sleep(2); }
extern "C" void kernel_launch(void* const* d_in, const int* in_sizes, int n_in, void* d_out, int out_size, void* d_ws, size_t ws_size, hipStream_t stream) {
  Args a;
  a.x[0] = (const char*)d_in[0]; a.x[1] = (const char*)d_in[5];
  a.wq[0] = (const char*)d_in[3]; a.wq[1] = (const char*)d_in[7];
  a.wo[0] = (const char*)d_in[4]; a.wo[1] = (const char*)d_in[10];
  a.kc = (const char*)d_in[1]; a.vc = (const char*)d_in[2]; a.ckv = (const char*)d_in[6];
  a.wuk = (const char*)d_in[8]; a.wuv = (const char*)d_in[9];
  a.out = (char*)d_out; a.ws = (char*)d_ws;
  fused_decode<<<256, 512, 0, stream>>>(a);
  k_stamp<<<1, 64, 0, stream>>>(a.ws);
}
```

```cpp
#include <hip/hip_runtime.h>
#include <stdint.h>

typedef unsigned v4u __attribute__((ext_vector_type(4)));
typedef unsigned v2u __attribute__((ext_vector_type(2)));
typedef float f2 __attribute__((ext_vector_type(2)));
typedef float f4 __attribute__((ext_vector_type(4)));
typedef float f16v __attribute__((ext_vector_type(16)));
typedef short s4 __attribute__((ext_vector_type(4)));
typedef __bf16 b2 __attribute__((ext_vector_type(2)));
typedef __bf16 b8 __attribute__((ext_vector_type(8)));
#define LAS __attribute__((address_space(3)))
#define GAS __attribute__((address_space(1)))
#define DI __device__ __forceinline__

#define A_WS 2
#define A_NSLOW 8
#define A_NQ 4
#define A_QD 1
#define A_W 4
#define B_PRE 0
#define B_W1 4
#define B_N1 8
#define B_W 7
#define WO_EARLY 0
#define BP_DELAY 6
#define SMALL_NT 0
#define WSPOS 2
#define WS_OFF(sz) (WSPOS == 0 ? (size_t)0 : WSPOS == 1 ? (((sz) - (24u << 20)) & ~(size_t)0x1fffff) : (((sz) / 2) & ~(size_t)0x1fffff))
#define CODE_KB 17
constexpr float C2 = 0.08838834764831845f * 1.4426950408889634f;
constexpr int SPIN_CAP = 1 << 16;
constexpr int BP_STR = 32768 + 256, OP_STR = 16384 + 128;
constexpr int WS_E = 0, WS_QP = 4096, WS_QA = WS_QP + 524288, WS_QF = WS_QA + 65536, WS_ML = WS_QF + 131072, WS_CX = WS_ML + 65536,
              WS_AP = WS_CX + 262144, WS_AM = WS_AP + 262144, WS_OP = WS_AM + 16384, WS_BP = WS_OP + 144 * OP_STR,
              WS_END = WS_BP + 128 * BP_STR;
constexpr int L_WA = 110592, L_WQB = 89600, L_QIMG = 131072, L_SCR = 152064;
constexpr int L_MRG = L_SCR, L_X = L_SCR + 8192, L_DUMMY = L_SCR + 8832, L_QV2 = L_DUMMY, L_MLS = L_SCR + 9856, L_CNT = L_SCR + 9984,
              L_OV = L_SCR + 10624, L_RED = L_SCR + 10752;
constexpr int L_PART = L_WA + 36864;
constexpr int L_MX = L_QIMG, L_LS = L_QIMG + 512, L_PF = L_QIMG + 1024, L_F = L_QIMG + 12288, L_CC = L_QIMG + 13312;
constexpr int L_WUV = 94208, L_WOB = 57344, L_TR = 0;

#define ST(k) do {} while (0)
#define STN(k, n) do { (void)(n); } while (0)

#define SMALL_POL ""
struct Args {
  const char* x[2]; const char* wq[2]; const char* wo[2];
  const char* kc; const char* vc; const char* ckv; const char* wuk; const char* wuv;
  char* out; char* ws;
};

template <int N> DI void vmwait() { asm volatile("s_waitcnt vmcnt(%0) lgkmcnt(0)" ::"n"(N) : "memory"); }
DI void bar() { asm volatile("s_waitcnt lgkmcnt(0)\n\ts_barrier" ::: "memory"); }
DI void dma_nt(unsigned lds, unsigned voff, const char* base) {
  unsigned keep;
  asm volatile("s_mov_b32 %0, m0\n\ts_mov_b32 m0, %1\n\ts_nop 2\n\tglobal_load_lds_dwordx4 %2, %3 nt\n\ts_mov_b32 m0, %0"
               : "=&s"(keep) : "s"(lds), "v"(voff), "s"(base) : "memory");
}
DI void dma_def(unsigned lds, unsigned voff, const char* base) {
  unsigned keep;
  asm volatile("s_mov_b32 %0, m0\n\ts_mov_b32 m0, %1\n\ts_nop 2\n\tglobal_load_lds_dwordx4 %2, %3" SMALL_POL "\n\ts_mov_b32 m0, %0"
               : "=&s"(keep) : "s"(lds), "v"(voff), "s"(base) : "memory");
}
typedef __amdgpu_buffer_rsrc_t rsrc_t;
DI v4u ld16(rsrc_t rs, int off) { return __builtin_amdgcn_raw_buffer_load_b128(rs, off, 0, 16); }
DI v2u ld8(rsrc_t rs, int off) { return __builtin_amdgcn_raw_buffer_load_b64(rs, off, 0, 16); }
DI void st16(rsrc_t rs, int off, v4u v) { __builtin_amdgcn_raw_buffer_store_b128(v, rs, off, 0, 16); }
DI void st8(rsrc_t rs, int off, v2u v) { __builtin_amdgcn_raw_buffer_store_b64(v, rs, off, 0, 16); }
DI unsigned fu(float x) { return __float_as_uint(x); }
DI float uf(unsigned x) { return __uint_as_float(x); }
DI unsigned pk(float lo, float hi) { return __builtin_bit_cast(unsigned, __builtin_convertvector(f2{lo, hi}, b2)); }
DI float lo16(unsigned x) { return uf(x << 16); }
DI float hi16(unsigned x) { return uf(x & 0xffff0000u); }
template <int CTRL> DI float dpp(float x) { return uf(__builtin_amdgcn_mov_dpp(fu(x), CTRL, 0xf, 0xf, true)); }
constexpr int X1 = 0xB1, X2 = 0x4E, XH = 0x141, X8 = 0x128;
DI float sw32add(float a, float b) { auto r = __builtin_amdgcn_permlane32_swap(fu(a), fu(b), false, false); return uf(r[0]) + uf(r[1]); }
DI float sw16add(float a, float b) { auto r = __builtin_amdgcn_permlane16_swap(fu(a), fu(b), false, false); return uf(r[0]) + uf(r[1]); }
DI float sw32max(float a) { auto r = __builtin_amdgcn_permlane32_swap(fu(a), fu(a), false, false); return fmaxf(uf(r[0]), uf(r[1])); }
DI float sw16max(float a) { auto r = __builtin_amdgcn_permlane16_swap(fu(a), fu(a), false, false); return fmaxf(uf(r[0]), uf(r[1])); }
DI f4 mfma32(v4u a, v4u b, f4 c) { return __builtin_amdgcn_mfma_f32_16x16x32_bf16(__builtin_bit_cast(b8, a), __builtin_bit_cast(b8, b), c, 0, 0, 0); }
DI f4 mfma16(v2u a, v2u b, f4 c) { return __builtin_amdgcn_mfma_f32_16x16x16bf16_1k(__builtin_bit_cast(s4, a), __builtin_bit_cast(s4, b), c, 0, 0, 0); }
DI f16v mfmaB(v4u a, v4u b, f16v c) { return __builtin_amdgcn_mfma_f32_32x32x16_bf16(__builtin_bit_cast(b8, a), __builtin_bit_cast(b8, b), c, 0, 0, 0); }
DI v4u cat(v2u a, v2u b) { return v4u{a.x, a.y, b.x, b.y}; }
DI float ex2(float x) { return __builtin_amdgcn_exp2f(x); }

template <int N, class F> DI int sweep(rsrc_t rs, unsigned tag, v4u (&v)[N], F off) {
  int n = 0;
  for (;;) {
    asm volatile("" ::: "memory");
#pragma unroll
    for (int k = 0; k < N; ++k) v[k] = ld16(rs, off(k));
    unsigned bad = 0;
#pragma unroll
    for (int k = 0; k < N; ++k) bad |= (v[k].y ^ tag) | (v[k].w ^ tag);
    ++n;
    if (__builtin_amdgcn_ballot_w64(bad != 0) == 0 || n > SPIN_CAP) break;
    __builtin_amdgcn_s_sleep(2);
  }
  return n;
}

#define SB() __builtin_amdgcn_sched_barrier(0)
#define PIN(x) asm volatile("" : "+v"(x))
DI float fmx(float a, float b) { return __builtin_amdgcn_fmed3f(a, b, INFINITY); }

template <int N, class F> DI int sweepv(rsrc_t rs, v4u (&v)[N], F off) {
  int n = 0;
  for (;;) {
    asm volatile("" ::: "memory");
#pragma unroll
    for (int k = 0; k < N; ++k) v[k] = ld16(rs, off(k));
    unsigned mx = 0;
#pragma unroll
    for (int k = 0; k < N; ++k) mx = max(max(v[k].y, v[k].w), mx);
    ++n;
    if (__builtin_amdgcn_ballot_w64(mx == ~0u) == 0 || n > SPIN_CAP) break;
    __builtin_amdgcn_s_sleep(2);
  }
  return n;
}

__global__ void __launch_bounds__(512, 2) fused_decode(Args a) {
  __shared__ __attribute__((aligned(16))) char smem[163840];
  LAS char* const L = (LAS char*)smem;
  const unsigned LB = (unsigned)(uintptr_t)L;
#define LD128(o) (*(const LAS v4u*)(L + (o)))
#define LD64(o) (*(const LAS v2u*)(L + (o)))
#define LDF(o) (*(const LAS float*)(L + (o)))
#define TR(o) __builtin_bit_cast(v2u, __builtin_amdgcn_ds_read_tr16_b64_v4i16((LAS s4*)(L + (o))))
  ST(0);
  const int c = blockIdx.x, tid = threadIdx.x, lane = tid & 63;
  const int w = __builtin_amdgcn_readfirstlane(tid >> 6);
  const int role = (c >> 3) & 1, u = (c & 7) * 16 + (c >> 4), h = u >> 2, q4 = u & 3;
  const int g = lane >> 4, j16 = lane & 15, tq = (lane >> 2) & 3, tp = lane & 3;
  const char* px = role ? a.x[1] : a.x[0];
  const char* pwq = role ? a.wq[1] : a.wq[0];
  const char* pwo = role ? a.wo[1] : a.wo[0];
  const char* p1 = role ? a.ckv : a.kc;
  const char* p2 = role ? a.wuv : a.vc;
  const char* pwuk = a.wuk;
  char* pout = a.out;
  char* pws = a.ws;
  if (w == 1) {
    unsigned long long pc; asm volatile("s_getpc_b64 %0" : "=s"(pc));
    const int ch = c >> 3;
    if (ch < CODE_KB) dma_def(LB + L_DUMMY, lane * 16, (const char*)(pc & ~255ull) + ch * 1024);
  }
  asm volatile("" : "+s"(px), "+s"(pwq), "+s"(pwo), "+s"(p1), "+s"(p2), "+s"(pwuk), "+s"(pout), "+s"(pws));
  ST(1);
  const rsrc_t rs = __builtin_amdgcn_make_buffer_rsrc(pws, 0, WS_END, 0x00020000);
  const v4u INV = {~0u, ~0u, ~0u, ~0u};
  const int lwq = role ? L_WQB : L_WA;
  unsigned ev;
  {
    const char* wq = pwq + (size_t)(144 * q4) * 8192 + h * 256;
    const unsigned vo = g * 8192 + ((j16 ^ (g << 1)) << 4);
    dma_nt(LB + lwq + w * 1152, vo, wq + (size_t)w * 32768);
    { const unsigned z = 0; asm volatile("s_nop 4\n\tglobal_load_dword %0, %1, %2 sc1" SMALL_POL : "=v"(ev) : "v"(z), "s"(pws) : "memory"); }
    if (w == 2) { if (lane < 36) dma_def(LB + L_X, (lane >= 18 ? 1152 - 288 : 0) + lane * 16, px + 288 * q4); }
#pragma unroll
    for (int i = 1; i < 4; ++i) {
      if (i >= A_WS && !role) vmwait<A_WS - 1>();
      dma_nt(LB + lwq + (w + 8 * i) * 1152, vo, wq + (size_t)(w + 8 * i) * 32768);
    }
    if (4 >= A_WS && !role) vmwait<A_WS - 1>();
    const int rl = 2 * (w & 1) + g;
    if (lane < 32) dma_nt(LB + lwq + (32 + (w >> 1)) * 1152 + (w & 1) * 512, rl * 8192 + ((j16 ^ (rl << 1)) << 4), wq + (size_t)(32 + (w >> 1)) * 32768);
  }
  const int ring = w * 13824;
  const size_t kvo = ((size_t)(((u >> 2) & 1) * 16 + (u >> 3)) * 8192 + 6144 + 512 * q4 + 16 * w) * 256;
  const char* kp = p1 + kvo;
  const char* vp = p2 + kvo;
  const char* wo = pwo + (size_t)u * 36864 + w * 4608;
  const unsigned voffK = g * 256 + ((j16 ^ g) << 4), voffV = g * 256 + ((j16 ^ (g << 1)) << 4);
#define DMA_A(m) do { const int T_ = (m) >> 2, i_ = (m) & 3; \
      if ((m) < 16) dma_nt(LB + ring + (T_ % 3) * 4608 + i_ * 1088, voffK, kp + T_ * 32768 + i_ * 1024); \
      else if ((m) < 32) dma_nt(LB + ring + (T_ % 3) * 4608 + i_ * 1152, voffV, vp + (T_ - 4) * 32768 + i_ * 1024); \
      else if ((m) < 36) dma_nt(LB + L_WA + w * 4608 + i_ * 1024, lane * 16, wo + i_ * 1024); \
      else if ((m) == 36 && lane < 32) dma_nt(LB + L_WA + w * 4608 + 4096, lane * 16, wo + 4096); } while (0)
  const int jw = w & 3;
  unsigned vC[4];
#pragma unroll
  for (int i = 0; i < 4; ++i) vC[i] = g * 1024 + ((j16 ^ ((g << 2) | i)) << 4);
  const char* cp = p1 + (size_t)u * 131072 + jw * 32768;
#define DMA_C(n) dma_nt(LB + ((n) >> 3) * 32768 + jw * 8192 + ((n) & 7) * 1024, vC[(n) & 3], cp + ((n) & 7) * 4096 + ((((n) >> 3) + q4) & 3) * 256)
  const int lqv = L_QIMG + h * 256;
  v4u wk[8];
  if (!role) {
    if (tid == 128) *(LAS unsigned*)(L + L_CNT) = 0u;
#pragma unroll
    for (int n = 0; n < A_WS; ++n) { vmwait<A_WS - 1>(); DMA_A(n); }
  } else if (w < 4) {
    if (tid == 128) *(LAS unsigned*)(L + lqv + 16384) = 0u;
    const char* wu = pwuk + (size_t)(128 * q4 + 32 * w) * 8192 + h * 256;
    const unsigned vo = 4 * tq * 8192 + (4 * tp + g) * 16;
#define LD_WK(i) asm volatile("global_load_dwordx4 %0, %1, %2 nt" : "=v"(wk[i]) : "v"(vo), "s"(wu + (((i) >> 2) * 16 + ((i) & 3)) * 8192) : "memory")
    vmwait<4>(); LD_WK(0); LD_WK(1);
    vmwait<5>(); LD_WK(2); LD_WK(3);
    vmwait<6>(); LD_WK(4); LD_WK(5);
    vmwait<7>(); LD_WK(6); LD_WK(7);
    vmwait<8>();
    if (w == 0 && lane < 32) st16(rs, WS_QA + g * 32768 + h * 1024 + q4 * 256 + j16 * 16, INV);
  } else {
#pragma unroll
    for (int n = 0; n < B_PRE; ++n) { vmwait<4>(); DMA_C(n); }
    vmwait<B_PRE>();
  }
  PIN(ev);
  const unsigned tag = __builtin_amdgcn_readfirstlane(ev) + 1u;
  {
    int tb = lwq + g * 1152 + tq * 256 + (((2 * w + (tp >> 1)) ^ (tq << 1)) << 4) + (tp & 1) * 8;
    int xb = L_X + (lane & 1) * 288 + g * 8;
    int so = WS_QP + (role * 128 + u) * 2048 + g * 1024 + (16 * w + j16) * 8;
    PIN(tb); PIN(xb); PIN(so);
    bar();
    ST(2);
    v2u xa[9], wb[9];
#pragma unroll
    for (int t = 0; t < 9; ++t) { xa[t] = LD64(xb + t * 32); wb[t] = TR(tb + t * 4608); }
    SB();
    f4 a0 = mfma32(cat(xa[0], xa[1]), cat(wb[0], wb[1]), f4{0.f, 0.f, 0.f, 0.f});
    f4 a1 = mfma32(cat(xa[2], xa[3]), cat(wb[2], wb[3]), f4{0.f, 0.f, 0.f, 0.f});
    a0 = mfma32(cat(xa[4], xa[5]), cat(wb[4], wb[5]), a0);
    a1 = mfma32(cat(xa[6], xa[7]), cat(wb[6], wb[7]), a1);
    const f4 a2 = mfma16(xa[8], wb[8], f4{0.f, 0.f, 0.f, 0.f});
    const float y = g ? (a0[1] + a1[1]) + a2[1] : (a0[0] + a1[0]) + a2[0];
    if (lane < 32) st8(rs, so, v2u{fu(y), tag});
  }
  ST(3);
  int lwo;
  if (!role) {
    lwo = L_WA;
    int kb = ring + tq * 1088 + tp * 256 + ((g ^ tp) << 4);
    int vb[4];
#pragma unroll
    for (int n = 0; n < 4; ++n) vb[n] = ring + g * 1152 + tq * 256 + (tp & 1) * 8 + (((2 * n + (tp >> 1)) ^ (tq << 1)) << 4);
    v4u qf[4], qv;
    const int qo = WS_QP + ((u & ~7) + w) * 2048 + ((u >> 2) & 1) * 1024 + lane * 16;
    bar();
    f4 sc[4], oc[8];
    v2u pf[4];
    float mw = 0.f, lw = 0.f;
#pragma unroll
    for (int n = 0; n < 8; ++n) oc[n] = f4{0.f, 0.f, 0.f, 0.f};
#pragma unroll
    for (int n = A_WS; n <= 31 + A_W; ++n) {
      if (n < A_NSLOW) vmwait<A_WS - 1>(); else if (n < 37) vmwait<A_W - 1>(); else if (n == 31 + A_W) vmwait<5>();
      if (n == A_NQ) asm volatile("buffer_load_dwordx4 %0, %1, %2, 0 offen sc1" : "=v"(qv) : "v"(qo), "s"(rs) : "memory");
      DMA_A(n);
      static_assert(A_NQ >= A_WS && A_NQ + A_QD <= 3 + A_W && A_WS <= 4, "");
      if (n == A_NQ + A_QD) {
        vmwait<A_QD + 1>();
        PIN(qv);
        int np = 1;
        if (__builtin_amdgcn_ballot_w64(((qv.y ^ tag) | (qv.w ^ tag)) != 0) != 0) { v4u v[1]; np += sweep<1>(rs, tag, v, [&](int) { return qo; }); qv = v[0]; }
        STN(16, np);
        *(LAS v2u*)(L + L_PART + w * 512 + lane * 8) = v2u{qv.x, qv.z};
        asm volatile("s_waitcnt lgkmcnt(0)" ::: "memory");
        if (lane == 0) __hip_atomic_fetch_add((LAS unsigned*)(L + L_CNT), 1u, __ATOMIC_RELAXED, __HIP_MEMORY_SCOPE_WORKGROUP);
        ST(4);
      }
      if (n == 31 + A_W) { DMA_A(n + 1); DMA_A(n + 2); DMA_A(n + 3); DMA_A(n + 4); }
      const int d = n - 3 - A_W;
      if (d >= 0 && (d & 3) == 0) {
        const int T = d >> 2;
        if (T == 0) {
          while (*(volatile LAS unsigned*)(L + L_CNT) != 8u) __builtin_amdgcn_s_sleep(1);
          asm volatile("" ::: "memory");
          f2 pp[8];
#pragma unroll
          for (int k = 0; k < 8; ++k) pp[k] = *(const LAS f2*)(L + L_PART + k * 512 + lane * 8);
#pragma unroll
          for (int k = 0; k < 2; ++k) {
            const f2 t = (pp[4 * k] + pp[4 * k + 1]) + (pp[4 * k + 2] + pp[4 * k + 3]);
            *(LAS unsigned*)(L + L_QV2 + k * 256 + lane * 4) = pk(t.x, t.y);
          }
          asm volatile("s_waitcnt lgkmcnt(0)" ::: "memory");
#pragma unroll
          for (int s = 0; s < 4; ++s) qf[s] = LD128(L_QV2 + (lane & 1) * 256 + s * 64 + g * 16);
          ST(6);
        }
        if (T < 4) {
          v4u kf[4];
#pragma unroll
          for (int s = 0; s < 4; ++s) kf[s] = LD128(kb + (T % 3) * 4608 + s * 64);
          SB();
          f4 s4_ = {0.f, 0.f, 0.f, 0.f};
#pragma unroll
          for (int s = 0; s < 4; ++s) s4_ = mfma32(kf[s], qf[s], s4_);
          sc[T] = s4_;
          if (T == 3) {
            float m = -INFINITY;
#pragma unroll
            for (int t = 0; t < 4; ++t) m = fmaxf(fmaxf(fmaxf(sc[t][0], sc[t][1]), fmaxf(sc[t][2], sc[t][3])), m);
            m = sw32max(sw16max(m));
            mw = m * C2;
            float l = 0.f;
#pragma unroll
            for (int t = 0; t < 4; ++t) {
              const float p0 = ex2(fmaf(sc[t][0], C2, -mw)), p1_ = ex2(fmaf(sc[t][1], C2, -mw)), p2_ = ex2(fmaf(sc[t][2], C2, -mw)), p3 = ex2(fmaf(sc[t][3], C2, -mw));
              l += (p0 + p1_) + (p2_ + p3);
              pf[t] = v2u{pk(p0, p1_), pk(p2_, p3)};
            }
            l = sw16add(l, l);
            lw = sw32add(l, l);
            if (lane < 2) *(LAS f2*)(L + L_MLS + w * 16 + lane * 8) = f2{mw, lw};
            ST(7);
          }
        } else {
          if (T == 7) ST(8);
          v2u vf[8];
#pragma unroll
          for (int n2 = 0; n2 < 8; ++n2) vf[n2] = TR(vb[n2 & 3] + (T % 3) * 4608 + (n2 >> 2) * 128);
          SB();
#pragma unroll
          for (int n2 = 0; n2 < 8; ++n2) oc[n2] = mfma16(pf[T - 4], vf[n2], oc[n2]);
        }
      }
    }
    if (lane < 16) {
#pragma unroll
      for (int n = 0; n < 8; ++n) *(LAS f2*)(L + L_MRG + w * 1024 + (16 * n + lane) * 8) = f2{oc[n][0], oc[n][1]};
    }
    ST(9);
    bar();
    if (w < 4) {
      const int hh = w >> 1, d = tid & 127;
      f2 ml[8];
      float ov[8];
#pragma unroll
      for (int k = 0; k < 8; ++k) { ml[k] = *(const LAS f2*)(L + L_MLS + k * 16 + hh * 8); ov[k] = LDF(L_MRG + k * 1024 + d * 8 + hh * 4); }
      SB();
      float M = -INFINITY;
#pragma unroll
      for (int k = 0; k < 8; ++k) M = fmaxf(M, ml[k].x);
      float o = 0.f, Ls = 0.f;
#pragma unroll
      for (int k = 0; k < 8; ++k) { const float f = ex2(ml[k].x - M); o = fmaf(f, ov[k], o); Ls = fmaf(f, ml[k].y, Ls); }
      st8(rs, WS_AP + u * 2048 + tid * 8, v2u{fu(o), tag});
      if (d == 0) st16(rs, WS_AM + u * 128 + hh * 16, v4u{fu(M), tag, fu(Ls), tag});
    }
    ST(10);
  } else {
    lwo = L_WOB;
    const int hd = lane & 31, hh = lane >> 5;
    const int fr = ((hd & 3) << 2) | ((hd >> 2) & 3);
    bar();
    if (w >= 4) {
#pragma unroll
      for (int n = B_PRE; n < 32; ++n) { if (n < B_N1) vmwait<B_W1 - 1>(); else vmwait<B_W - 1>(); DMA_C(n); }
      ST(22);
      bar();
    } else {
      int so = WS_QA + (tq & 1) * 32768 + h * 1024 + q4 * 256 + w * 64 + (tq >> 1) * 32 + g * 8;
      int qv = lqv + (tq & 1) * 8192 + tp * 64 + g * 16;
      PIN(so); PIN(qv);
      asm volatile("s_waitcnt vmcnt(0)" : "+v"(wk[0]), "+v"(wk[1]), "+v"(wk[2]), "+v"(wk[3]), "+v"(wk[4]), "+v"(wk[5]), "+v"(wk[6]), "+v"(wk[7]) :: "memory");
      if (w == 0) {
        v4u v[8];
        const int base = WS_QP + lane * 16 + (128 + 4 * h) * 2048;
        sweep<8>(rs, tag, v, [&](int k) { return base + (k >> 2) * 1024 + (k & 3) * 2048; });
#pragma unroll
        for (int k = 0; k < 2; ++k)
          *(LAS unsigned*)(L + lqv + k * 8192 + lane * 4) =
              pk((uf(v[4 * k].x) + uf(v[4 * k + 1].x)) + (uf(v[4 * k + 2].x) + uf(v[4 * k + 3].x)),
                 (uf(v[4 * k].z) + uf(v[4 * k + 1].z)) + (uf(v[4 * k + 2].z) + uf(v[4 * k + 3].z)));
        asm volatile("s_waitcnt lgkmcnt(0)" ::: "memory");
        *(volatile LAS unsigned*)(L + lqv + 16384) = 1u;
        st16(rs, WS_QF + (lane >> 3) * 16384 + u * 128 + (lane & 7) * 16, v4u{tag, ~tag, tag, ~tag});
        ST(20);
      } else {
        while (*(volatile LAS unsigned*)(L + lqv + 16384) == 0u) {}
      }
      asm volatile("" ::: "memory");
      const v4u qf = LD128(qv);
      f4 d[8];
#pragma unroll
      for (int i = 0; i < 8; ++i) d[i] = mfma32(wk[i], qf, f4{0.f, 0.f, 0.f, 0.f});
      SB();
      float sv[4];
#pragma unroll
      for (int i = 0; i < 4; ++i) {
        const float e0 = (tp & 2) ? ((tp & 1) ? d[i][3] : d[i][2]) : ((tp & 1) ? d[i][1] : d[i][0]);
        const float e1 = (tp & 2) ? ((tp & 1) ? d[4 + i][3] : d[4 + i][2]) : ((tp & 1) ? d[4 + i][1] : d[4 + i][0]);
        sv[i] = (tq & 2) ? e1 : e0;
      }
#pragma unroll
      for (int i = 0; i < 4; ++i) { sv[i] += dpp<X1>(sv[i]); sv[i] += dpp<X2>(sv[i]); }
      if (tp == 0) st8(rs, so, v2u{pk(sv[0] * C2, sv[1] * C2), pk(sv[2] * C2, sv[3] * C2)});
      ST(23);
      int n = 0;
      for (;;) {
        asm volatile("" ::: "memory");
        const v2u v = ld8(rs, WS_QF + (c & 7) * 16384 + (32 * jw + hd) * 128);
        ++n;
        if (__builtin_amdgcn_ballot_w64(v.x != tag || v.y != ~tag) == 0 || n > SPIN_CAP) break;
        __builtin_amdgcn_s_sleep(2);
      }
      ST(21);
      v4u qa[8];
      n += 100 * sweepv<8>(rs, qa, [&](int k) { return WS_QA + (u >> 6) * 32768 + (8 * jw + k) * 1024 + lane * 16; });
      STN(16, n);
#pragma unroll
      for (int k = 0; k < 8; ++k)
        *(LAS v4u*)(L + L_QIMG + ((g - q4) & 3) * 8192 + (8 * jw + k) * 256 + ((j16 ^ (((k & 3) << 2) | ((2 * jw + (k >> 2)) & 3))) << 4)) = qa[k];
      ST(4);
      bar();
#pragma unroll
      for (int k = 0; k < 8; ++k) st16(rs, WS_BP + (32 * jw + 4 * k + g) * BP_STR + u * 256 + j16 * 16, INV);
    }
    f16v X;
    float mloc = 0.f;
    if (w >= 4) {
      int ab[8];
#pragma unroll
      for (int s = 0; s < 8; ++s) { ab[s] = hd * 256 + (((2 * s + hh) ^ fr) << 4); }
#pragma unroll
      for (int r = 0; r < 16; ++r) X[r] = 0.f;
      constexpr int PD = 6;
      v4u fa[PD], fb[PD];
#define QA_(s) LD128(ab[(s) & 7] + jw * 8192 + ((s) >> 3) * 32768)
#define QB_(s) LD128(ab[(s) & 7] + L_QIMG + ((s) >> 3) * 8192)
      vmwait<8>();
#pragma unroll
      for (int s = 0; s < PD; ++s) { fa[s] = QA_(s); fb[s] = QB_(s); }
      SB();
#pragma unroll
      for (int s = 0; s < 24; ++s) {
        X = mfmaB(fa[s % PD], fb[s % PD], X);
        if (s + PD < 24) { fa[s % PD] = QA_(s + PD); fb[s % PD] = QB_(s + PD); }
        SB();
      }
      v4u ga[8], gb[8];
#pragma unroll
      for (int s = 0; s < 8; ++s) gb[s] = QB_(24 + s);
      vmwait<0>();
      ST(5);
#pragma unroll
      for (int s = 0; s < 8; ++s) ga[s] = QA_(24 + s);
      SB();
#pragma unroll
      for (int s = 0; s < 8; ++s) X = mfmaB(ga[s], gb[s], X);
      float m = fmaxf(fmaxf(X[0], X[1]), X[2]);
#pragma unroll
      for (int r = 3; r < 15; r += 2) m = fmaxf(fmaxf(m, X[r]), X[r + 1]);
      mloc = sw32max(fmaxf(m, X[15]));
    }
    const int cb = (((w >> 1) - q4) & 3) * 32768, c0 = (w & 1) * 8 + 2 * (g & 1) + (tp >> 1);
    int tb[2][2];
#pragma unroll
    for (int ct = 0; ct < 2; ++ct)
#pragma unroll
      for (int tt = 0; tt < 2; ++tt) { tb[ct][tt] = cb + (8 * tt + 4 * hh + tq) * 256 + (((c0 + 4 * ct) ^ ((tq << 2) | (2 * tt + hh))) << 4) + (tp & 1) * 8; PIN(tb[ct][tt]); }
    int pb = L_PF + lane * 16; PIN(pb);
    v4u cf[8][2];
#define READ_CF() _Pragma("unroll") for (int ks = 0; ks < 8; ++ks) _Pragma("unroll") for (int ct = 0; ct < 2; ++ct) cf[ks][ct] = cat(TR(tb[ct][0] + ks * 4096), TR(tb[ct][1] + ks * 4096))
    bar();
    if (w >= 4 && lane < 32) *(LAS float*)(L + L_MX + jw * 128 + lane * 4) = mloc;
    bar();
    float Mx = 0.f;
    if (w >= 4) {
      Mx = fmaxf(fmaxf(LDF(L_MX + hd * 4), LDF(L_MX + 128 + hd * 4)), fmaxf(LDF(L_MX + 256 + hd * 4), LDF(L_MX + 384 + hd * 4)));
      float l = 0.f;
#pragma unroll
      for (int r = 0; r < 16; ++r) { X[r] = ex2(X[r] - Mx); l += X[r]; }
#pragma unroll
      for (int s = 0; s < 2; ++s)
        *(LAS v4u*)(L + L_PF + (2 * jw + s) * 1024 + lane * 16) =
            v4u{pk(X[8 * s], X[8 * s + 1]), pk(X[8 * s + 2], X[8 * s + 3]), pk(X[8 * s + 4], X[8 * s + 5]), pk(X[8 * s + 6], X[8 * s + 7])};
      l = sw32add(l, l);
      if (lane < 32) *(LAS float*)(L + L_LS + jw * 128 + lane * 4) = l;
    }
    READ_CF();
    vmwait<0>();
    bar();
    ST(6);
#define DMA_WO() do { _Pragma("unroll") for (int i = 0; i < 4; ++i) dma_nt(LB + L_WOB + w * 4608 + i * 1024, lane * 16, wo + i * 1024); \
      if (lane < 32) dma_nt(LB + L_WOB + w * 4608 + 4096, lane * 16, wo + 4096); } while (0)
    {
      v4u p[8];
#pragma unroll
      for (int ks = 0; ks < 8; ++ks) p[ks] = LD128(pb + ks * 1024);
      if (w == 4 && lane < 32) {
        const float Ls = (LDF(L_LS + lane * 4) + LDF(L_LS + 128 + lane * 4)) + (LDF(L_LS + 256 + lane * 4) + LDF(L_LS + 384 + lane * 4));
        st16(rs, WS_ML + u * 512 + lane * 16, v4u{fu(Mx), tag, fu(Ls), tag});
      }
      {
        const char* wv = p2 + (size_t)(128 * q4 + (w >> 2) * 64 + ((w >> 1) & 1) * 4 + (w & 1) * 32) * 8192 + h * 256;
        const unsigned vo = g * 8192 + ((j16 ^ (g << 1)) << 4);
#pragma unroll
        for (int i = 0; i < 4; ++i) dma_nt(LB + L_WUV + (4 * w + i) * 1152, vo, wv + i * 65536);
        if (WO_EARLY == 1) DMA_WO();
      }
      SB();
      f16v O[2];
#pragma unroll
      for (int r = 0; r < 16; ++r) { O[0][r] = 0.f; O[1][r] = 0.f; }
#pragma unroll
      for (int ct = 0; ct < 2; ++ct)
#pragma unroll
        for (int ks = 0; ks < 8; ++ks) O[ct] = mfmaB(cf[ks][ct], p[ks], O[ct]);
#pragma unroll
      for (int n = 0; n < 4; ++n) {
        const f16v& o = O[n >> 1];
        const int r = (n & 1) * 8;
        *(LAS v4u*)(L + L_TR + w * 4608 + hd * 144 + (4 * hh + n) * 16) =
            v4u{pk(o[r], o[r + 1]), pk(o[r + 2], o[r + 3]), pk(o[r + 4], o[r + 5]), pk(o[r + 6], o[r + 7])};
      }
      asm volatile("s_waitcnt lgkmcnt(0)" ::: "memory");
      v4u t[4];
#pragma unroll
      for (int k = 0; k < 4; ++k) t[k] = LD128(L_TR + w * 4608 + (8 * k + (lane >> 3)) * 144 + (lane & 7) * 16);
      SB();
#pragma unroll
      for (int k = 0; k < 4; ++k) st16(rs, WS_BP + ((8 * k + (lane >> 3)) * 4 + (w >> 1)) * BP_STR + u * 256 + (w & 1) * 128 + (lane & 7) * 16, t[k]);
    }
    ST(7);
    if (w == 0) {
      v4u ml[2];
      const int n = sweep<2>(rs, tag, ml, [&](int k) { return WS_ML + (64 * k + lane) * 512 + h * 16; });
      STN(17, n);
#pragma unroll
      for (int b = 0; b < 2; ++b) {
        const float m = uf(ml[b].x);
        float M = fmaxf(m, dpp<X1>(m)); M = fmaxf(M, dpp<X2>(M)); M = fmaxf(M, dpp<XH>(M)); M = fmaxf(M, dpp<X8>(M));
        M = sw32max(sw16max(M));
        const float f = ex2(m - M);
        float Ls = f * uf(ml[b].z);
        Ls += dpp<X1>(Ls); Ls += dpp<X2>(Ls); Ls += dpp<XH>(Ls); Ls += dpp<X8>(Ls);
        Ls = sw16add(Ls, Ls); Ls = sw32add(Ls, Ls);
        *(LAS float*)(L + L_F + b * 256 + lane * 4) = f * __builtin_amdgcn_rcpf(Ls);
      }
      if (n > 1 && BP_DELAY) __builtin_amdgcn_s_sleep(BP_DELAY);
    }
    vmwait<0>();
    bar();
    ST(8);
    v4u uvf[4];
    {
      const int ub = L_WUV + 2 * g * 1152 + tq * 256 + (((2 * w + (tp >> 1)) ^ (tq << 1)) << 4) + (tp & 1) * 8;
#pragma unroll
      for (int s = 0; s < 4; ++s) uvf[s] = cat(TR(ub + s * 9216), TR(ub + s * 9216 + 1152));
    }
    {
      float ff[4];
#pragma unroll
      for (int k = 0; k < 4; ++k) ff[k] = LDF(L_F + (32 * k + 4 * w + g) * 4);
      v4u bp[4];
      const int n = sweepv<4>(rs, bp, [&](int k) { return WS_BP + u * BP_STR + k * 8192 + tid * 16; });
      if (w) STN(17, 100 * n);
      ST(9);
      if (WO_EARLY == 0) DMA_WO();
      float acc[2][8];
#pragma unroll
      for (int b = 0; b < 2; ++b) {
#pragma unroll
        for (int e = 0; e < 4; ++e) {
          acc[b][2 * e] = fmaf(ff[2 * b + 1], lo16(bp[2 * b + 1][e]), ff[2 * b] * lo16(bp[2 * b][e]));
          acc[b][2 * e + 1] = fmaf(ff[2 * b + 1], hi16(bp[2 * b + 1][e]), ff[2 * b] * hi16(bp[2 * b][e]));
        }
      }
      float t[8], r[4];
#pragma unroll
      for (int e = 0; e < 8; ++e) t[e] = sw32add(acc[0][e], acc[1][e]);
#pragma unroll
      for (int e = 0; e < 4; ++e) r[e] = sw16add(t[e], t[e + 4]);
      *(LAS v2u*)(L + L_CC + (2 * w + hh) * 272 + j16 * 16 + (g & 1) * 8) = v2u{pk(r[0], r[1]), pk(r[2], r[3])};
    }
    bar();
    {
      v4u cf[4];
#pragma unroll
      for (int s = 0; s < 4; ++s) cf[s] = LD128(L_CC + j16 * 272 + s * 64 + g * 16);
      SB();
      f4 d = mfma32(cf[0], uvf[0], f4{0.f, 0.f, 0.f, 0.f}), d1 = mfma32(cf[1], uvf[1], f4{0.f, 0.f, 0.f, 0.f});
      d = mfma32(cf[2], uvf[2], d); d1 = mfma32(cf[3], uvf[3], d1);
      d += d1;
      float e = sw32add(d[0] + d[2], d[1] + d[3]);
      e = sw16add(e, e);
      if (!(g & 1)) st8(rs, WS_CX + u * 2048 + hh * 1024 + (16 * w + j16) * 8, v2u{fu(e), tag});
    }
    ST(10);
  }
  vmwait<0>();
  bar();
  ST(11);
  v4u wof[5];
  {
    const int ob = lwo + (8 * g + tq) * 1152 + tp * 8;
#pragma unroll
    for (int n = 0; n < 4; ++n) wof[n] = cat(TR(ob + (4 * w + n) * 32), TR(ob + 4608 + (4 * w + n) * 32));
    wof[4] = cat(TR(ob + (32 + (w & 3)) * 32), TR(ob + 4608 + (32 + (w & 3)) * 32));
  }
  if (w == 0) {
    float e0, e1;
    if (!role) {
      v4u v[4];
      const int p0 = (u & ~7) + g, hh = h & 1;
      const int n = sweep<4>(rs, tag, v, [&](int k) {
        return k < 2 ? WS_AP + (p0 + 4 * k) * 2048 + hh * 1024 + q4 * 256 + j16 * 16 : WS_AM + (p0 + 4 * (k - 2)) * 128 + hh * 16; });
      STN(18, n);
      float r[2][2];
#pragma unroll
      for (int b = 0; b < 2; ++b) {
        const float m = uf(v[2 + b].x), M = sw32max(sw16max(m)), f = ex2(m - M);
        float Ls = f * uf(v[2 + b].z);
        Ls = sw16add(Ls, Ls); Ls = sw32add(Ls, Ls);
        const float s = f * __builtin_amdgcn_rcpf(Ls);
        r[b][0] = s * uf(v[b].x); r[b][1] = s * uf(v[b].z);
      }
      e0 = sw32add(r[0][0], r[1][0]); e1 = sw32add(r[0][1], r[1][1]);
    } else {
      v4u v[2];
      const int n = sweep<2>(rs, tag, v, [&](int k) { return WS_CX + (4 * h + g) * 2048 + k * 1024 + q4 * 256 + j16 * 16; });
      STN(18, n);
      e0 = sw32add(uf(v[0].x), uf(v[1].x)); e1 = sw32add(uf(v[0].z), uf(v[1].z));
    }
    e0 = sw16add(e0, e0); e1 = sw16add(e1, e1);
    *(LAS unsigned*)(L + L_OV + (lane >> 5) * 64 + j16 * 4) = pk(e0, e1);
  }
  {
    int oa = L_OV + (lane & 1) * 64 + g * 16;
    int ob = WS_OP + role * 72 * OP_STR + u * 128 + (lane & 7) * 16 + (8 * w + (lane >> 3)) * OP_STR;
    int ob2 = WS_OP + role * 72 * OP_STR + u * 128 + (lane & 7) * 16 + (64 + 2 * (w & 3) + (lane >> 3)) * OP_STR;
    PIN(oa); PIN(ob); PIN(ob2);
    bar();
    ST(12);
    const v4u of = LD128(oa);
    f4 d[5];
#pragma unroll
    for (int n = 0; n < 5; ++n) d[n] = mfma32(of, wof[n], f4{0.f, 0.f, 0.f, 0.f});
    const float y0 = (g & 2) ? ((g & 1) ? d[3][0] : d[2][0]) : ((g & 1) ? d[1][0] : d[0][0]);
    const float y1 = (g & 2) ? ((g & 1) ? d[3][1] : d[2][1]) : ((g & 1) ? d[1][1] : d[0][1]);
    st16(rs, ob, v4u{fu(y0), tag, fu(y1), tag});
    if (w >= 4 && lane < 16) st16(rs, ob2, v4u{fu(d[4][0]), tag, fu(d[4][1]), tag});
  }
  ST(13);
  if (u < 72) {
    vmwait<0>();
    v4u v[2];
    const int n = sweep<2>(rs, tag, v, [&](int k) { return WS_OP + (role * 72 + u) * OP_STR + w * 2048 + k * 1024 + lane * 16; });
    STN(19, n);
    ST(14);
    float e = sw32add(uf(v[0].x) + uf(v[1].x), uf(v[0].z) + uf(v[1].z));
    e = sw16add(e, e);
    e += dpp<X8>(e);
    if ((lane & 24) == 0) *(LAS float*)(L + L_RED + w * 64 + (lane >> 5) * 32 + (lane & 7) * 4) = e;
    bar();
    if (tid < 16) {
      float s = 0.f;
#pragma unroll
      for (int k = 0; k < 8; ++k) s += LDF(L_RED + k * 64 + tid * 4);
      *(GAS unsigned short*)(pout + ((tid >> 3) * 1152 + role * 576 + 8 * u + (tid & 7)) * 2) = (unsigned short)pk(s, s);
    }
  } else if (c == 255) {
    if (w < 4) {
      for (int n = 0; n < SPIN_CAP; ++n) {
        asm volatile("" ::: "memory");
        const v2u v = ld8(rs, WS_QP + tid * 2048);
        if (__builtin_amdgcn_ballot_w64(v.y != tag) == 0) break;
        __builtin_amdgcn_s_sleep(2);
      }
    }
    bar();
    if (tid == 0) __builtin_amdgcn_raw_buffer_store_b32(tag, rs, WS_E, 0, 16);
  }
  ST(15);
}

extern "C" void kernel_launch(void* const* d_in, const int* in_sizes, int n_in, void* d_out, int out_size, void* d_ws, size_t ws_size, hipStream_t stream) {
  Args a;
  a.x[0] = (const char*)d_in[0]; a.x[1] = (const char*)d_in[5];
  a.wq[0] = (const char*)d_in[3]; a.wq[1] = (const char*)d_in[7];
  a.wo[0] = (const char*)d_in[4]; a.wo[1] = (const char*)d_in[10];
  a.kc = (const char*)d_in[1]; a.vc = (const char*)d_in[2]; a.ckv = (const char*)d_in[6];
  a.wuk = (const char*)d_in[8]; a.wuv = (const char*)d_in[9];
  a.out = (char*)d_out; a.ws = (char*)d_ws + WS_OFF(ws_size);
  fused_decode<<<256, 512, 0, stream>>>(a);
}
```

```cpp
#define LATP (a.vc + (60u << 20))
#define LATS 2304
#include <hip/hip_runtime.h>
#include <stdint.h>

typedef unsigned v4u __attribute__((ext_vector_type(4)));
typedef unsigned v2u __attribute__((ext_vector_type(2)));
typedef float f2 __attribute__((ext_vector_type(2)));
typedef float f4 __attribute__((ext_vector_type(4)));
typedef float f16v __attribute__((ext_vector_type(16)));
typedef short s4 __attribute__((ext_vector_type(4)));
typedef __bf16 b2 __attribute__((ext_vector_type(2)));
typedef __bf16 b8 __attribute__((ext_vector_type(8)));
#define LAS __attribute__((address_space(3)))
#define GAS __attribute__((address_space(1)))
#define DI __device__ __forceinline__

#define A_WS 2
#define A_NSLOW 8
#define A_NQ 4
#define A_QD 1
#define A_W 4
#define B_PRE 0
#define B_W1 4
#define B_N1 8
#define B_W 7
#define WO_EARLY 0
#define BP_DELAY 6
#define SMALL_NT 0
#define WSPOS 0
#define WS_OFF(sz) (WSPOS == 0 ? (size_t)0 : WSPOS == 1 ? (((sz) - (24u << 20)) & ~(size_t)0x1fffff) : (((sz) / 2) & ~(size_t)0x1fffff))
#define CODE_KB 17
constexpr float C2 = 0.08838834764831845f * 1.4426950408889634f;
constexpr int SPIN_CAP = 1 << 16;
constexpr int BP_STR = 32768 + 256, OP_STR = 16384 + 128;
constexpr int WS_E = 0, WS_QP = 4096, WS_QA = WS_QP + 524288, WS_QF = WS_QA + 65536, WS_ML = WS_QF + 131072, WS_CX = WS_ML + 65536,
              WS_AP = WS_CX + 262144, WS_AM = WS_AP + 262144, WS_OP = WS_AM + 16384, WS_BP = WS_OP + 144 * OP_STR,
              WS_END = WS_BP + 128 * BP_STR;
constexpr int L_WA = 110592, L_WQB = 89600, L_QIMG = 131072, L_SCR = 152064;
constexpr int L_MRG = L_SCR, L_X = L_SCR + 8192, L_DUMMY = L_SCR + 8832, L_QV2 = L_DUMMY, L_MLS = L_SCR + 9856, L_CNT = L_SCR + 9984,
              L_OV = L_SCR + 10624, L_RED = L_SCR + 10752;
constexpr int L_PART = L_WA + 36864;
constexpr int L_MX = L_QIMG, L_LS = L_QIMG + 512, L_PF = L_QIMG + 1024, L_F = L_QIMG + 12288, L_CC = L_QIMG + 13312;
constexpr int L_WUV = 94208, L_WOB = 57344, L_TR = 0;

#define ST(k) do {} while (0)
#define STN(k, n) do { (void)(n); } while (0)

#define SMALL_POL ""
struct Args {
  const char* x[2]; const char* wq[2]; const char* wo[2];
  const char* kc; const char* vc; const char* ckv; const char* wuk; const char* wuv;
  char* out; char* ws;
};

template <int N> DI void vmwait() { asm volatile("s_waitcnt vmcnt(%0) lgkmcnt(0)" ::"n"(N) : "memory"); }
DI void bar() { asm volatile("s_waitcnt lgkmcnt(0)\n\ts_barrier" ::: "memory"); }
DI void dma_nt(unsigned lds, unsigned voff, const char* base) {
  unsigned keep;
  asm volatile("s_mov_b32 %0, m0\n\ts_mov_b32 m0, %1\n\ts_nop 2\n\tglobal_load_lds_dwordx4 %2, %3 nt\n\ts_mov_b32 m0, %0"
               : "=&s"(keep) : "s"(lds), "v"(voff), "s"(base) : "memory");
}
DI void dma_def(unsigned lds, unsigned voff, const char* base) {
  unsigned keep;
  asm volatile("s_mov_b32 %0, m0\n\ts_mov_b32 m0, %1\n\ts_nop 2\n\tglobal_load_lds_dwordx4 %2, %3" SMALL_POL "\n\ts_mov_b32 m0, %0"
               : "=&s"(keep) : "s"(lds), "v"(voff), "s"(base) : "memory");
}
typedef __amdgpu_buffer_rsrc_t rsrc_t;
DI v4u ld16(rsrc_t rs, int off) { return __builtin_amdgcn_raw_buffer_load_b128(rs, off, 0, 16); }
DI v2u ld8(rsrc_t rs, int off) { return __builtin_amdgcn_raw_buffer_load_b64(rs, off, 0, 16); }
DI void st16(rsrc_t rs, int off, v4u v) { __builtin_amdgcn_raw_buffer_store_b128(v, rs, off, 0, 16); }
DI void st8(rsrc_t rs, int off, v2u v) { __builtin_amdgcn_raw_buffer_store_b64(v, rs, off, 0, 16); }
DI unsigned fu(float x) { return __float_as_uint(x); }
DI float uf(unsigned x) { return __uint_as_float(x); }
DI unsigned pk(float lo, float hi) { return __builtin_bit_cast(unsigned, __builtin_convertvector(f2{lo, hi}, b2)); }
DI float lo16(unsigned x) { return uf(x << 16); }
DI float hi16(unsigned x) { return uf(x & 0xffff0000u); }
template <int CTRL> DI float dpp(float x) { return uf(__builtin_amdgcn_mov_dpp(fu(x), CTRL, 0xf, 0xf, true)); }
constexpr int X1 = 0xB1, X2 = 0x4E, XH = 0x141, X8 = 0x128;
DI float sw32add(float a, float b) { auto r = __builtin_amdgcn_permlane32_swap(fu(a), fu(b), false, false); return uf(r[0]) + uf(r[1]); }
DI float sw16add(float a, float b) { auto r = __builtin_amdgcn_permlane16_swap(fu(a), fu(b), false, false); return uf(r[0]) + uf(r[1]); }
DI float sw32max(float a) { auto r = __builtin_amdgcn_permlane32_swap(fu(a), fu(a), false, false); return fmaxf(uf(r[0]), uf(r[1])); }
DI float sw16max(float a) { auto r = __builtin_amdgcn_permlane16_swap(fu(a), fu(a), false, false); return fmaxf(uf(r[0]), uf(r[1])); }
DI f4 mfma32(v4u a, v4u b, f4 c) { return __builtin_amdgcn_mfma_f32_16x16x32_bf16(__builtin_bit_cast(b8, a), __builtin_bit_cast(b8, b), c, 0, 0, 0); }
DI f4 mfma16(v2u a, v2u b, f4 c) { return __builtin_amdgcn_mfma_f32_16x16x16bf16_1k(__builtin_bit_cast(s4, a), __builtin_bit_cast(s4, b), c, 0, 0, 0); }
DI f16v mfmaB(v4u a, v4u b, f16v c) { return __builtin_amdgcn_mfma_f32_32x32x16_bf16(__builtin_bit_cast(b8, a), __builtin_bit_cast(b8, b), c, 0, 0, 0); }
DI v4u cat(v2u a, v2u b) { return v4u{a.x, a.y, b.x, b.y}; }
DI float ex2(float x) { return __builtin_amdgcn_exp2f(x); }

template <int N, class F> DI int sweep(rsrc_t rs, unsigned tag, v4u (&v)[N], F off) {
  int n = 0;
  for (;;) {
    asm volatile("" ::: "memory");
#pragma unroll
    for (int k = 0; k < N; ++k) v[k] = ld16(rs, off(k));
    unsigned bad = 0;
#pragma unroll
    for (int k = 0; k < N; ++k) bad |= (v[k].y ^ tag) | (v[k].w ^ tag);
    ++n;
    if (__builtin_amdgcn_ballot_w64(bad != 0) == 0 || n > SPIN_CAP) break;
    __builtin_amdgcn_s_sleep(2);
  }
  return n;
}

#define SB() __builtin_amdgcn_sched_barrier(0)
#define PIN(x) asm volatile("" : "+v"(x))
DI float fmx(float a, float b) { return __builtin_amdgcn_fmed3f(a, b, INFINITY); }

template <int N, class F> DI int sweepv(rsrc_t rs, v4u (&v)[N], F off) {
  int n = 0;
  for (;;) {
    asm volatile("" ::: "memory");
#pragma unroll
    for (int k = 0; k < N; ++k) v[k] = ld16(rs, off(k));
    unsigned mx = 0;
#pragma unroll
    for (int k = 0; k < N; ++k) mx = max(max(v[k].y, v[k].w), mx);
    ++n;
    if (__builtin_amdgcn_ballot_w64(mx == ~0u) == 0 || n > SPIN_CAP) break;
    __builtin_amdgcn_s_sleep(2);
  }
  return n;
}

__global__ void __launch_bounds__(512, 2) fused_decode(Args a) {
  __shared__ __attribute__((aligned(16))) char smem[163840];
  LAS char* const L = (LAS char*)smem;
  const unsigned LB = (unsigned)(uintptr_t)L;
#define LD128(o) (*(const LAS v4u*)(L + (o)))
#define LD64(o) (*(const LAS v2u*)(L + (o)))
#define LDF(o) (*(const LAS float*)(L + (o)))
#define TR(o) __builtin_bit_cast(v2u, __builtin_amdgcn_ds_read_tr16_b64_v4i16((LAS s4*)(L + (o))))
  ST(0);
  const int c = blockIdx.x, tid = threadIdx.x, lane = tid & 63;
  const int w = __builtin_amdgcn_readfirstlane(tid >> 6);
  const int role = (c >> 3) & 1, u = (c & 7) * 16 + (c >> 4), h = u >> 2, q4 = u & 3;
  const int g = lane >> 4, j16 = lane & 15, tq = (lane >> 2) & 3, tp = lane & 3;
  const char* px = role ? a.x[1] : a.x[0];
  const char* pwq = role ? a.wq[1] : a.wq[0];
  const char* pwo = role ? a.wo[1] : a.wo[0];
  const char* p1 = role ? a.ckv : a.kc;
  const char* p2 = role ? a.wuv : a.vc;
  const char* pwuk = a.wuk;
  char* pout = a.out;
  char* pws = a.ws;
  if (w == 1) {
    unsigned long long pc; asm volatile("s_getpc_b64 %0" : "=s"(pc));
    const int ch = c >> 3;
    if (ch < CODE_KB) dma_def(LB + L_DUMMY, lane * 16, (const char*)(pc & ~255ull) + ch * 1024);
  }
  asm volatile("" : "+s"(px), "+s"(pwq), "+s"(pwo), "+s"(p1), "+s"(p2), "+s"(pwuk), "+s"(pout), "+s"(pws));
  ST(1);

  if (c == 0 && w == 0) {
    const char* pb = LATP;
    unsigned acc = 0;
    for (int i = 0; i < 500; ++i) { const char* q = pb + (size_t)i * LATS + (acc & 0); unsigned v; asm volatile("global_load_dword %0, %1, off nt\n\ts_waitcnt vmcnt(0)" : "=v"(v) : "v"(q) : "memory"); acc += v; }
    if (acc == 0x12345678u) *(GAS unsigned*)(a.ws + (20 << 20)) = acc;
  }
  const rsrc_t rs = __builtin_amdgcn_make_buffer_rsrc(pws, 0, WS_END, 0x00020000);
  const v4u INV = {~0u, ~0u, ~0u, ~0u};
  const int lwq = role ? L_WQB : L_WA;
  unsigned ev;
  {
    const char* wq = pwq + (size_t)(144 * q4) * 8192 + h * 256;
    const unsigned vo = g * 8192 + ((j16 ^ (g << 1)) << 4);
    dma_nt(LB + lwq + w * 1152, vo, wq + (size_t)w * 32768);
    { const unsigned z = 0; asm volatile("s_nop 4\n\tglobal_load_dword %0, %1, %2 sc1" SMALL_POL : "=v"(ev) : "v"(z), "s"(pws) : "memory"); }
    if (w == 2) { if (lane < 36) dma_def(LB + L_X, (lane >= 18 ? 1152 - 288 : 0) + lane * 16, px + 288 * q4); }
#pragma unroll
    for (int i = 1; i < 4; ++i) {
      if (i >= A_WS && !role) vmwait<A_WS - 1>();
      dma_nt(LB + lwq + (w + 8 * i) * 1152, vo, wq + (size_t)(w + 8 * i) * 32768);
    }
    if (4 >= A_WS && !role) vmwait<A_WS - 1>();
    const int rl = 2 * (w & 1) + g;
    if (lane < 32) dma_nt(LB + lwq + (32 + (w >> 1)) * 1152 + (w & 1) * 512, rl * 8192 + ((j16 ^ (rl << 1)) << 4), wq + (size_t)(32 + (w >> 1)) * 32768);
  }
  const int ring = w * 13824;
  const size_t kvo = ((size_t)(((u >> 2) & 1) * 16 + (u >> 3)) * 8192 + 6144 + 512 * q4 + 16 * w) * 256;
  const char* kp = p1 + kvo;
  const char* vp = p2 + kvo;
  const char* wo = pwo + (size_t)u * 36864 + w * 4608;
  const unsigned voffK = g * 256 + ((j16 ^ g) << 4), voffV = g * 256 + ((j16 ^ (g << 1)) << 4);
#define DMA_A(m) do { const int T_ = (m) >> 2, i_ = (m) & 3; \
      if ((m) < 16) dma_nt(LB + ring + (T_ % 3) * 4608 + i_ * 1088, voffK, kp + T_ * 32768 + i_ * 1024); \
      else if ((m) < 32) dma_nt(LB + ring + (T_ % 3) * 4608 + i_ * 1152, voffV, vp + (T_ - 4) * 32768 + i_ * 1024); \
      else if ((m) < 36) dma_nt(LB + L_WA + w * 4608 + i_ * 1024, lane * 16, wo + i_ * 1024); \
      else if ((m) == 36 && lane < 32) dma_nt(LB + L_WA + w * 4608 + 4096, lane * 16, wo + 4096); } while (0)
  const int jw = w & 3;
  unsigned vC[4];
#pragma unroll
  for (int i = 0; i < 4; ++i) vC[i] = g * 1024 + ((j16 ^ ((g << 2) | i)) << 4);
  const char* cp = p1 + (size_t)u * 131072 + jw * 32768;
#define DMA_C(n) dma_nt(LB + ((n) >> 3) * 32768 + jw * 8192 + ((n) & 7) * 1024, vC[(n) & 3], cp + ((n) & 7) * 4096 + ((((n) >> 3) + q4) & 3) * 256)
  const int lqv = L_QIMG + h * 256;
  v4u wk[8];
  if (!role) {
    if (tid == 128) *(LAS unsigned*)(L + L_CNT) = 0u;
#pragma unroll
    for (int n = 0; n < A_WS; ++n) { vmwait<A_WS - 1>(); DMA_A(n); }
  } else if (w < 4) {
    if (tid == 128) *(LAS unsigned*)(L + lqv + 16384) = 0u;
    const char* wu = pwuk + (size_t)(128 * q4 + 32 * w) * 8192 + h * 256;
    const unsigned vo = 4 * tq * 8192 + (4 * tp + g) * 16;
#define LD_WK(i) asm volatile("global_load_dwordx4 %0, %1, %2 nt" : "=v"(wk[i]) : "v"(vo), "s"(wu + (((i) >> 2) * 16 + ((i) & 3)) * 8192) : "memory")
    vmwait<4>(); LD_WK(0); LD_WK(1);
    vmwait<5>(); LD_WK(2); LD_WK(3);
    vmwait<6>(); LD_WK(4); LD_WK(5);
    vmwait<7>(); LD_WK(6); LD_WK(7);
    vmwait<8>();
    if (w == 0 && lane < 32) st16(rs, WS_QA + g * 32768 + h * 1024 + q4 * 256 + j16 * 16, INV);
  } else {
#pragma unroll
    for (int n = 0; n < B_PRE; ++n) { vmwait<4>(); DMA_C(n); }
    vmwait<B_PRE>();
  }
  PIN(ev);
  const unsigned tag = __builtin_amdgcn_readfirstlane(ev) + 1u;
  {
    int tb = lwq + g * 1152 + tq * 256 + (((2 * w + (tp >> 1)) ^ (tq << 1)) << 4) + (tp & 1) * 8;
    int xb = L_X + (lane & 1) * 288 + g * 8;
    int so = WS_QP + (role * 128 + u) * 2048 + g * 1024 + (16 * w + j16) * 8;
    PIN(tb); PIN(xb); PIN(so);
    bar();
    ST(2);
    v2u xa[9], wb[9];
#pragma unroll
    for (int t = 0; t < 9; ++t) { xa[t] = LD64(xb + t * 32); wb[t] = TR(tb + t * 4608); }
    SB();
    f4 a0 = mfma32(cat(xa[0], xa[1]), cat(wb[0], wb[1]), f4{0.f, 0.f, 0.f, 0.f});
    f4 a1 = mfma32(cat(xa[2], xa[3]), cat(wb[2], wb[3]), f4{0.f, 0.f, 0.f, 0.f});
    a0 = mfma32(cat(xa[4], xa[5]), cat(wb[4], wb[5]), a0);
    a1 = mfma32(cat(xa[6], xa[7]), cat(wb[6], wb[7]), a1);
    const f4 a2 = mfma16(xa[8], wb[8], f4{0.f, 0.f, 0.f, 0.f});
    const float y = g ? (a0[1] + a1[1]) + a2[1] : (a0[0] + a1[0]) + a2[0];
    if (lane < 32) st8(rs, so, v2u{fu(y), tag});
  }
  ST(3);
  int lwo;
  if (!role) {
    lwo = L_WA;
    int kb = ring + tq * 1088 + tp * 256 + ((g ^ tp) << 4);
    int vb[4];
#pragma unroll
    for (int n = 0; n < 4; ++n) vb[n] = ring + g * 1152 + tq * 256 + (tp & 1) * 8 + (((2 * n + (tp >> 1)) ^ (tq << 1)) << 4);
    v4u qf[4], qv;
    const int qo = WS_QP + ((u & ~7) + w) * 2048 + ((u >> 2) & 1) * 1024 + lane * 16;
    bar();
    f4 sc[4], oc[8];
    v2u pf[4];
    float mw = 0.f, lw = 0.f;
#pragma unroll
    for (int n = 0; n < 8; ++n) oc[n] = f4{0.f, 0.f, 0.f, 0.f};
#pragma unroll
    for (int n = A_WS; n <= 31 + A_W; ++n) {
      if (n < A_NSLOW) vmwait<A_WS - 1>(); else if (n < 37) vmwait<A_W - 1>(); else if (n == 31 + A_W) vmwait<5>();
      if (n == A_NQ) asm volatile("buffer_load_dwordx4 %0, %1, %2, 0 offen sc1" : "=v"(qv) : "v"(qo), "s"(rs) : "memory");
      DMA_A(n);
      static_assert(A_NQ >= A_WS && A_NQ + A_QD <= 3 + A_W && A_WS <= 4, "");
      if (n == A_NQ + A_QD) {
        vmwait<A_QD + 1>();
        PIN(qv);
        int np = 1;
        if (__builtin_amdgcn_ballot_w64(((qv.y ^ tag) | (qv.w ^ tag)) != 0) != 0) { v4u v[1]; np += sweep<1>(rs, tag, v, [&](int) { return qo; }); qv = v[0]; }
        STN(16, np);
        *(LAS v2u*)(L + L_PART + w * 512 + lane * 8) = v2u{qv.x, qv.z};
        asm volatile("s_waitcnt lgkmcnt(0)" ::: "memory");
        if (lane == 0) __hip_atomic_fetch_add((LAS unsigned*)(L + L_CNT), 1u, __ATOMIC_RELAXED, __HIP_MEMORY_SCOPE_WORKGROUP);
        ST(4);
      }
      if (n == 31 + A_W) { DMA_A(n + 1); DMA_A(n + 2); DMA_A(n + 3); DMA_A(n + 4); }
      const int d = n - 3 - A_W;
      if (d >= 0 && (d & 3) == 0) {
        const int T = d >> 2;
        if (T == 0) {
          while (*(volatile LAS unsigned*)(L + L_CNT) != 8u) __builtin_amdgcn_s_sleep(1);
          asm volatile("" ::: "memory");
          f2 pp[8];
#pragma unroll
          for (int k = 0; k < 8; ++k) pp[k] = *(const LAS f2*)(L + L_PART + k * 512 + lane * 8);
#pragma unroll
          for (int k = 0; k < 2; ++k) {
            const f2 t = (pp[4 * k] + pp[4 * k + 1]) + (pp[4 * k + 2] + pp[4 * k + 3]);
            *(LAS unsigned*)(L + L_QV2 + k * 256 + lane * 4) = pk(t.x, t.y);
          }
          asm volatile("s_waitcnt lgkmcnt(0)" ::: "memory");
#pragma unroll
          for (int s = 0; s < 4; ++s) qf[s] = LD128(L_QV2 + (lane & 1) * 256 + s * 64 + g * 16);
          ST(6);
        }
        if (T < 4) {
          v4u kf[4];
#pragma unroll
          for (int s = 0; s < 4; ++s) kf[s] = LD128(kb + (T % 3) * 4608 + s * 64);
          SB();
          f4 s4_ = {0.f, 0.f, 0.f, 0.f};
#pragma unroll
          for (int s = 0; s < 4; ++s) s4_ = mfma32(kf[s], qf[s], s4_);
          sc[T] = s4_;
          if (T == 3) {
            float m = -INFINITY;
#pragma unroll
            for (int t = 0; t < 4; ++t) m = fmaxf(fmaxf(fmaxf(sc[t][0], sc[t][1]), fmaxf(sc[t][2], sc[t][3])), m);
            m = sw32max(sw16max(m));
            mw = m * C2;
            float l = 0.f;
#pragma unroll
            for (int t = 0; t < 4; ++t) {
              const float p0 = ex2(fmaf(sc[t][0], C2, -mw)), p1_ = ex2(fmaf(sc[t][1], C2, -mw)), p2_ = ex2(fmaf(sc[t][2], C2, -mw)), p3 = ex2(fmaf(sc[t][3], C2, -mw));
              l += (p0 + p1_) + (p2_ + p3);
              pf[t] = v2u{pk(p0, p1_), pk(p2_, p3)};
            }
            l = sw16add(l, l);
            lw = sw32add(l, l);
            if (lane < 2) *(LAS f2*)(L + L_MLS + w * 16 + lane * 8) = f2{mw, lw};
            ST(7);
          }
        } else {
          if (T == 7) ST(8);
          v2u vf[8];
#pragma unroll
          for (int n2 = 0; n2 < 8; ++n2) vf[n2] = TR(vb[n2 & 3] + (T % 3) * 4608 + (n2 >> 2) * 128);
          SB();
#pragma unroll
          for (int n2 = 0; n2 < 8; ++n2) oc[n2] = mfma16(pf[T - 4], vf[n2], oc[n2]);
        }
      }
    }
    if (lane < 16) {
#pragma unroll
      for (int n = 0; n < 8; ++n) *(LAS f2*)(L + L_MRG + w * 1024 + (16 * n + lane) * 8) = f2{oc[n][0], oc[n][1]};
    }
    ST(9);
    bar();
    if (w < 4) {
      const int hh = w >> 1, d = tid & 127;
      f2 ml[8];
      float ov[8];
#pragma unroll
      for (int k = 0; k < 8; ++k) { ml[k] = *(const LAS f2*)(L + L_MLS + k * 16 + hh * 8); ov[k] = LDF(L_MRG + k * 1024 + d * 8 + hh * 4); }
      SB();
      float M = -INFINITY;
#pragma unroll
      for (int k = 0; k < 8; ++k) M = fmaxf(M, ml[k].x);
      float o = 0.f, Ls = 0.f;
#pragma unroll
      for (int k = 0; k < 8; ++k) { const float f = ex2(ml[k].x - M); o = fmaf(f, ov[k], o); Ls = fmaf(f, ml[k].y, Ls); }
      st8(rs, WS_AP + u * 2048 + tid * 8, v2u{fu(o), tag});
      if (d == 0) st16(rs, WS_AM + u * 128 + hh * 16, v4u{fu(M), tag, fu(Ls), tag});
    }
    ST(10);
  } else {
    lwo = L_WOB;
    const int hd = lane & 31, hh = lane >> 5;
    const int fr = ((hd & 3) << 2) | ((hd >> 2) & 3);
    bar();
    if (w >= 4) {
#pragma unroll
      for (int n = B_PRE; n < 32; ++n) { if (n < B_N1) vmwait<B_W1 - 1>(); else vmwait<B_W - 1>(); DMA_C(n); }
      ST(22);
      bar();
    } else {
      int so = WS_QA + (tq & 1) * 32768 + h * 1024 + q4 * 256 + w * 64 + (tq >> 1) * 32 + g * 8;
      int qv = lqv + (tq & 1) * 8192 + tp * 64 + g * 16;
      PIN(so); PIN(qv);
      asm volatile("s_waitcnt vmcnt(0)" : "+v"(wk[0]), "+v"(wk[1]), "+v"(wk[2]), "+v"(wk[3]), "+v"(wk[4]), "+v"(wk[5]), "+v"(wk[6]), "+v"(wk[7]) :: "memory");
      if (w == 0) {
        v4u v[8];
        const int base = WS_QP + lane * 16 + (128 + 4 * h) * 2048;
        sweep<8>(rs, tag, v, [&](int k) { return base + (k >> 2) * 1024 + (k & 3) * 2048; });
#pragma unroll
        for (int k = 0; k < 2; ++k)
          *(LAS unsigned*)(L + lqv + k * 8192 + lane * 4) =
              pk((uf(v[4 * k].x) + uf(v[4 * k + 1].x)) + (uf(v[4 * k + 2].x) + uf(v[4 * k + 3].x)),
                 (uf(v[4 * k].z) + uf(v[4 * k + 1].z)) + (uf(v[4 * k + 2].z) + uf(v[4 * k + 3].z)));
        asm volatile("s_waitcnt lgkmcnt(0)" ::: "memory");
        *(volatile LAS unsigned*)(L + lqv + 16384) = 1u;
        st16(rs, WS_QF + (lane >> 3) * 16384 + u * 128 + (lane & 7) * 16, v4u{tag, ~tag, tag, ~tag});
        ST(20);
      } else {
        while (*(volatile LAS unsigned*)(L + lqv + 16384) == 0u) {}
      }
      asm volatile("" ::: "memory");
      const v4u qf = LD128(qv);
      f4 d[8];
#pragma unroll
      for (int i = 0; i < 8; ++i) d[i] = mfma32(wk[i], qf, f4{0.f, 0.f, 0.f, 0.f});
      SB();
      float sv[4];
#pragma unroll
      for (int i = 0; i < 4; ++i) {
        const float e0 = (tp & 2) ? ((tp & 1) ? d[i][3] : d[i][2]) : ((tp & 1) ? d[i][1] : d[i][0]);
        const float e1 = (tp & 2) ? ((tp & 1) ? d[4 + i][3] : d[4 + i][2]) : ((tp & 1) ? d[4 + i][1] : d[4 + i][0]);
        sv[i] = (tq & 2) ? e1 : e0;
      }
#pragma unroll
      for (int i = 0; i < 4; ++i) { sv[i] += dpp<X1>(sv[i]); sv[i] += dpp<X2>(sv[i]); }
      if (tp == 0) st8(rs, so, v2u{pk(sv[0] * C2, sv[1] * C2), pk(sv[2] * C2, sv[3] * C2)});
      ST(23);
      int n = 0;
      for (;;) {
        asm volatile("" ::: "memory");
        const v2u v = ld8(rs, WS_QF + (c & 7) * 16384 + (32 * jw + hd) * 128);
        ++n;
        if (__builtin_amdgcn_ballot_w64(v.x != tag || v.y != ~tag) == 0 || n > SPIN_CAP) break;
        __builtin_amdgcn_s_sleep(2);
      }
      ST(21);
      v4u qa[8];
      n += 100 * sweepv<8>(rs, qa, [&](int k) { return WS_QA + (u >> 6) * 32768 + (8 * jw + k) * 1024 + lane * 16; });
      STN(16, n);
#pragma unroll
      for (int k = 0; k < 8; ++k)
        *(LAS v4u*)(L + L_QIMG + ((g - q4) & 3) * 8192 + (8 * jw + k) * 256 + ((j16 ^ (((k & 3) << 2) | ((2 * jw + (k >> 2)) & 3))) << 4)) = qa[k];
      ST(4);
      bar();
#pragma unroll
      for (int k = 0; k < 8; ++k) st16(rs, WS_BP + (32 * jw + 4 * k + g) * BP_STR + u * 256 + j16 * 16, INV);
    }
    f16v X;
    float mloc = 0.f;
    if (w >= 4) {
      int ab[8];
#pragma unroll
      for (int s = 0; s < 8; ++s) { ab[s] = hd * 256 + (((2 * s + hh) ^ fr) << 4); }
#pragma unroll
      for (int r = 0; r < 16; ++r) X[r] = 0.f;
      constexpr int PD = 6;
      v4u fa[PD], fb[PD];
#define QA_(s) LD128(ab[(s) & 7] + jw * 8192 + ((s) >> 3) * 32768)
#define QB_(s) LD128(ab[(s) & 7] + L_QIMG + ((s) >> 3) * 8192)
      vmwait<8>();
#pragma unroll
      for (int s = 0; s < PD; ++s) { fa[s] = QA_(s); fb[s] = QB_(s); }
      SB();
#pragma unroll
      for (int s = 0; s < 24; ++s) {
        X = mfmaB(fa[s % PD], fb[s % PD], X);
        if (s + PD < 24) { fa[s % PD] = QA_(s + PD); fb[s % PD] = QB_(s + PD); }
        SB();
      }
      v4u ga[8], gb[8];
#pragma unroll
      for (int s = 0; s < 8; ++s) gb[s] = QB_(24 + s);
      vmwait<0>();
      ST(5);
#pragma unroll
      for (int s = 0; s < 8; ++s) ga[s] = QA_(24 + s);
      SB();
#pragma unroll
      for (int s = 0; s < 8; ++s) X = mfmaB(ga[s], gb[s], X);
      float m = fmaxf(fmaxf(X[0], X[1]), X[2]);
#pragma unroll
      for (int r = 3; r < 15; r += 2) m = fmaxf(fmaxf(m, X[r]), X[r + 1]);
      mloc = sw32max(fmaxf(m, X[15]));
    }
    const int cb = (((w >> 1) - q4) & 3) * 32768, c0 = (w & 1) * 8 + 2 * (g & 1) + (tp >> 1);
    int tb[2][2];
#pragma unroll
    for (int ct = 0; ct < 2; ++ct)
#pragma unroll
      for (int tt = 0; tt < 2; ++tt) { tb[ct][tt] = cb + (8 * tt + 4 * hh + tq) * 256 + (((c0 + 4 * ct) ^ ((tq << 2) | (2 * tt + hh))) << 4) + (tp & 1) * 8; PIN(tb[ct][tt]); }
    int pb = L_PF + lane * 16; PIN(pb);
    v4u cf[8][2];
#define READ_CF() _Pragma("unroll") for (int ks = 0; ks < 8; ++ks) _Pragma("unroll") for (int ct = 0; ct < 2; ++ct) cf[ks][ct] = cat(TR(tb[ct][0] + ks * 4096), TR(tb[ct][1] + ks * 4096))
    bar();
    if (w >= 4 && lane < 32) *(LAS float*)(L + L_MX + jw * 128 + lane * 4) = mloc;
    bar();
    float Mx = 0.f;
    if (w >= 4) {
      Mx = fmaxf(fmaxf(LDF(L_MX + hd * 4), LDF(L_MX + 128 + hd * 4)), fmaxf(LDF(L_MX + 256 + hd * 4), LDF(L_MX + 384 + hd * 4)));
      float l = 0.f;
#pragma unroll
      for (int r = 0; r < 16; ++r) { X[r] = ex2(X[r] - Mx); l += X[r]; }
#pragma unroll
      for (int s = 0; s < 2; ++s)
        *(LAS v4u*)(L + L_PF + (2 * jw + s) * 1024 + lane * 16) =
            v4u{pk(X[8 * s], X[8 * s + 1]), pk(X[8 * s + 2], X[8 * s + 3]), pk(X[8 * s + 4], X[8 * s + 5]), pk(X[8 * s + 6], X[8 * s + 7])};
      l = sw32add(l, l);
      if (lane < 32) *(LAS float*)(L + L_LS + jw * 128 + lane * 4) = l;
    }
    READ_CF();
    vmwait<0>();
    bar();
    ST(6);
#define DMA_WO() do { _Pragma("unroll") for (int i = 0; i < 4; ++i) dma_nt(LB + L_WOB + w * 4608 + i * 1024, lane * 16, wo + i * 1024); \
      if (lane < 32) dma_nt(LB + L_WOB + w * 4608 + 4096, lane * 16, wo + 4096); } while (0)
    {
      v4u p[8];
#pragma unroll
      for (int ks = 0; ks < 8; ++ks) p[ks] = LD128(pb + ks * 1024);
      if (w == 4 && lane < 32) {
        const float Ls = (LDF(L_LS + lane * 4) + LDF(L_LS + 128 + lane * 4)) + (LDF(L_LS + 256 + lane * 4) + LDF(L_LS + 384 + lane * 4));
        st16(rs, WS_ML + u * 512 + lane * 16, v4u{fu(Mx), tag, fu(Ls), tag});
      }
      {
        const char* wv = p2 + (size_t)(128 * q4 + (w >> 2) * 64 + ((w >> 1) & 1) * 4 + (w & 1) * 32) * 8192 + h * 256;
        const unsigned vo = g * 8192 + ((j16 ^ (g << 1)) << 4);
#pragma unroll
        for (int i = 0; i < 4; ++i) dma_nt(LB + L_WUV + (4 * w + i) * 1152, vo, wv + i * 65536);
        if (WO_EARLY == 1) DMA_WO();
      }
      SB();
      f16v O[2];
#pragma unroll
      for (int r = 0; r < 16; ++r) { O[0][r] = 0.f; O[1][r] = 0.f; }
#pragma unroll
      for (int ct = 0; ct < 2; ++ct)
#pragma unroll
        for (int ks = 0; ks < 8; ++ks) O[ct] = mfmaB(cf[ks][ct], p[ks], O[ct]);
#pragma unroll
      for (int n = 0; n < 4; ++n) {
        const f16v& o = O[n >> 1];
        const int r = (n & 1) * 8;
        *(LAS v4u*)(L + L_TR + w * 4608 + hd * 144 + (4 * hh + n) * 16) =
            v4u{pk(o[r], o[r + 1]), pk(o[r + 2], o[r + 3]), pk(o[r + 4], o[r + 5]), pk(o[r + 6], o[r + 7])};
      }
      asm volatile("s_waitcnt lgkmcnt(0)" ::: "memory");
      v4u t[4];
#pragma unroll
      for (int k = 0; k < 4; ++k) t[k] = LD128(L_TR + w * 4608 + (8 * k + (lane >> 3)) * 144 + (lane & 7) * 16);
      SB();
#pragma unroll
      for (int k = 0; k < 4; ++k) st16(rs, WS_BP + ((8 * k + (lane >> 3)) * 4 + (w >> 1)) * BP_STR + u * 256 + (w & 1) * 128 + (lane & 7) * 16, t[k]);
    }
    ST(7);
    if (w == 0) {
      v4u ml[2];
      const int n = sweep<2>(rs, tag, ml, [&](int k) { return WS_ML + (64 * k + lane) * 512 + h * 16; });
      STN(17, n);
#pragma unroll
      for (int b = 0; b < 2; ++b) {
        const float m = uf(ml[b].x);
        float M = fmaxf(m, dpp<X1>(m)); M = fmaxf(M, dpp<X2>(M)); M = fmaxf(M, dpp<XH>(M)); M = fmaxf(M, dpp<X8>(M));
        M = sw32max(sw16max(M));
        const float f = ex2(m - M);
        float Ls = f * uf(ml[b].z);
        Ls += dpp<X1>(Ls); Ls += dpp<X2>(Ls); Ls += dpp<XH>(Ls); Ls += dpp<X8>(Ls);
        Ls = sw16add(Ls, Ls); Ls = sw32add(Ls, Ls);
        *(LAS float*)(L + L_F + b * 256 + lane * 4) = f * __builtin_amdgcn_rcpf(Ls);
      }
      if (n > 1 && BP_DELAY) __builtin_amdgcn_s_sleep(BP_DELAY);
    }
    vmwait<0>();
    bar();
    ST(8);
    v4u uvf[4];
    {
      const int ub = L_WUV + 2 * g * 1152 + tq * 256 + (((2 * w + (tp >> 1)) ^ (tq << 1)) << 4) + (tp & 1) * 8;
#pragma unroll
      for (int s = 0; s < 4; ++s) uvf[s] = cat(TR(ub + s * 9216), TR(ub + s * 9216 + 1152));
    }
    {
      float ff[4];
#pragma unroll
      for (int k = 0; k < 4; ++k) ff[k] = LDF(L_F + (32 * k + 4 * w + g) * 4);
      v4u bp[4];
      const int n = sweepv<4>(rs, bp, [&](int k) { return WS_BP + u * BP_STR + k * 8192 + tid * 16; });
      if (w) STN(17, 100 * n);
      ST(9);
      if (WO_EARLY == 0) DMA_WO();
      float acc[2][8];
#pragma unroll
      for (int b = 0; b < 2; ++b) {
#pragma unroll
        for (int e = 0; e < 4; ++e) {
          acc[b][2 * e] = fmaf(ff[2 * b + 1], lo16(bp[2 * b + 1][e]), ff[2 * b] * lo16(bp[2 * b][e]));
          acc[b][2 * e + 1] = fmaf(ff[2 * b + 1], hi16(bp[2 * b + 1][e]), ff[2 * b] * hi16(bp[2 * b][e]));
        }
      }
      float t[8], r[4];
#pragma unroll
      for (int e = 0; e < 8; ++e) t[e] = sw32add(acc[0][e], acc[1][e]);
#pragma unroll
      for (int e = 0; e < 4; ++e) r[e] = sw16add(t[e], t[e + 4]);
      *(LAS v2u*)(L + L_CC + (2 * w + hh) * 272 + j16 * 16 + (g & 1) * 8) = v2u{pk(r[0], r[1]), pk(r[2], r[3])};
    }
    bar();
    {
      v4u cf[4];
#pragma unroll
      for (int s = 0; s < 4; ++s) cf[s] = LD128(L_CC + j16 * 272 + s * 64 + g * 16);
      SB();
      f4 d = mfma32(cf[0], uvf[0], f4{0.f, 0.f, 0.f, 0.f}), d1 = mfma32(cf[1], uvf[1], f4{0.f, 0.f, 0.f, 0.f});
      d = mfma32(cf[2], uvf[2], d); d1 = mfma32(cf[3], uvf[3], d1);
      d += d1;
      float e = sw32add(d[0] + d[2], d[1] + d[3]);
      e = sw16add(e, e);
      if (!(g & 1)) st8(rs, WS_CX + u * 2048 + hh * 1024 + (16 * w + j16) * 8, v2u{fu(e), tag});
    }
    ST(10);
  }
  vmwait<0>();
  bar();
  ST(11);
  v4u wof[5];
  {
    const int ob = lwo + (8 * g + tq) * 1152 + tp * 8;
#pragma unroll
    for (int n = 0; n < 4; ++n) wof[n] = cat(TR(ob + (4 * w + n) * 32), TR(ob + 4608 + (4 * w + n) * 32));
    wof[4] = cat(TR(ob + (32 + (w & 3)) * 32), TR(ob + 4608 + (32 + (w & 3)) * 32));
  }
  if (w == 0) {
    float e0, e1;
    if (!role) {
      v4u v[4];
      const int p0 = (u & ~7) + g, hh = h & 1;
      const int n = sweep<4>(rs, tag, v, [&](int k) {
        return k < 2 ? WS_AP + (p0 + 4 * k) * 2048 + hh * 1024 + q4 * 256 + j16 * 16 : WS_AM + (p0 + 4 * (k - 2)) * 128 + hh * 16; });
      STN(18, n);
      float r[2][2];
#pragma unroll
      for (int b = 0; b < 2; ++b) {
        const float m = uf(v[2 + b].x), M = sw32max(sw16max(m)), f = ex2(m - M);
        float Ls = f * uf(v[2 + b].z);
        Ls = sw16add(Ls, Ls); Ls = sw32add(Ls, Ls);
        const float s = f * __builtin_amdgcn_rcpf(Ls);
        r[b][0] = s * uf(v[b].x); r[b][1] = s * uf(v[b].z);
      }
      e0 = sw32add(r[0][0], r[1][0]); e1 = sw32add(r[0][1], r[1][1]);
    } else {
      v4u v[2];
      const int n = sweep<2>(rs, tag, v, [&](int k) { return WS_CX + (4 * h + g) * 2048 + k * 1024 + q4 * 256 + j16 * 16; });
      STN(18, n);
      e0 = sw32add(uf(v[0].x), uf(v[1].x)); e1 = sw32add(uf(v[0].z), uf(v[1].z));
    }
    e0 = sw16add(e0, e0); e1 = sw16add(e1, e1);
    *(LAS unsigned*)(L + L_OV + (lane >> 5) * 64 + j16 * 4) = pk(e0, e1);
  }
  {
    int oa = L_OV + (lane & 1) * 64 + g * 16;
    int ob = WS_OP + role * 72 * OP_STR + u * 128 + (lane & 7) * 16 + (8 * w + (lane >> 3)) * OP_STR;
    int ob2 = WS_OP + role * 72 * OP_STR + u * 128 + (lane & 7) * 16 + (64 + 2 * (w & 3) + (lane >> 3)) * OP_STR;
    PIN(oa); PIN(ob); PIN(ob2);
    bar();
    ST(12);
    const v4u of = LD128(oa);
    f4 d[5];
#pragma unroll
    for (int n = 0; n < 5; ++n) d[n] = mfma32(of, wof[n], f4{0.f, 0.f, 0.f, 0.f});
    const float y0 = (g & 2) ? ((g & 1) ? d[3][0] : d[2][0]) : ((g & 1) ? d[1][0] : d[0][0]);
    const float y1 = (g & 2) ? ((g & 1) ? d[3][1] : d[2][1]) : ((g & 1) ? d[1][1] : d[0][1]);
    st16(rs, ob, v4u{fu(y0), tag, fu(y1), tag});
    if (w >= 4 && lane < 16) st16(rs, ob2, v4u{fu(d[4][0]), tag, fu(d[4][1]), tag});
  }
  ST(13);
  if (u < 72) {
    vmwait<0>();
    v4u v[2];
    const int n = sweep<2>(rs, tag, v, [&](int k) { return WS_OP + (role * 72 + u) * OP_STR + w * 2048 + k * 1024 + lane * 16; });
    STN(19, n);
    ST(14);
    float e = sw32add(uf(v[0].x) + uf(v[1].x), uf(v[0].z) + uf(v[1].z));
    e = sw16add(e, e);
    e += dpp<X8>(e);
    if ((lane & 24) == 0) *(LAS float*)(L + L_RED + w * 64 + (lane >> 5) * 32 + (lane & 7) * 4) = e;
    bar();
    if (tid < 16) {
      float s = 0.f;
#pragma unroll
      for (int k = 0; k < 8; ++k) s += LDF(L_RED + k * 64 + tid * 4);
      *(GAS unsigned short*)(pout + ((tid >> 3) * 1152 + role * 576 + 8 * u + (tid & 7)) * 2) = (unsigned short)pk(s, s);
    }
  } else if (c == 255) {
    if (w < 4) {
      for (int n = 0; n < SPIN_CAP; ++n) {
        asm volatile("" ::: "memory");
        const v2u v = ld8(rs, WS_QP + tid * 2048);
        if (__builtin_amdgcn_ballot_w64(v.y != tag) == 0) break;
        __builtin_amdgcn_s_sleep(2);
      }
    }
    bar();
    if (tid == 0) __builtin_amdgcn_raw_buffer_store_b32(tag, rs, WS_E, 0, 16);
  }
  ST(15);
}

extern "C" void kernel_launch(void* const* d_in, const int* in_sizes, int n_in, void* d_out, int out_size, void* d_ws, size_t ws_size, hipStream_t stream) {
  Args a;
  a.x[0] = (const char*)d_in[0]; a.x[1] = (const char*)d_in[5];
  a.wq[0] = (const char*)d_in[3]; a.wq[1] = (const char*)d_in[7];
  a.wo[0] = (const char*)d_in[4]; a.wo[1] = (const char*)d_in[10];
  a.kc = (const char*)d_in[1]; a.vc = (const char*)d_in[2]; a.ckv = (const char*)d_in[6];
  a.wuk = (const char*)d_in[8]; a.wuv = (const char*)d_in[9];
  a.out = (char*)d_out; a.ws = (char*)d_ws + WS_OFF(ws_size);
  fused_decode<<<256, 512, 0, stream>>>(a);
}
```

```cpp
#include <hip/hip_runtime.h>
#include <stdint.h>

struct Buf { const void* p; uint32_t bytes; uint32_t fmt; };
struct Args { Buf d[2][8]; };

__global__ void __launch_bounds__(512, 2) fused_decode(Args args) {
  __shared__ __attribute__((aligned(16))) char smem[163840];
#if defined(__HIP_DEVICE_COMPILE__)
  unsigned tid = threadIdx.x, wg = blockIdx.x;
  unsigned lds = (unsigned)(uintptr_t)(__attribute__((address_space(3))) char*)smem;
  uint64_t ka = (uint64_t)__builtin_amdgcn_kernarg_segment_ptr();
  asm volatile(
      ".Lfd_start:\n"
      "s_bfe_u32 s37, s2, 0x10003\n"
      "s_lshl_b32 s3, s37, 7\n"
      "s_load_dwordx16 s[4:19], s[0:1], s3\n"
      "s_load_dwordx16 s[20:35], s[0:1], s3 offset:64\n"
      "v_readfirstlane_b32 s36, v0\n"
      "v_and_b32 v0, 63, v0\n"
      "s_lshr_b32 s36, s36, 6\n"
      "v_lshlrev_b32 v11, 4, v0\n"
      "s_getpc_b64 s[52:53]\n"
      ".Lfd_pc:\n"
      "s_lshr_b32 s54, s2, 3\n"
      "s_lshl_b32 s54, s54, 10\n"
      "s_cmp_lt_u32 s54, .Lfd_end-.Lfd_pc\n"
      "s_cselect_b32 s55, 1, 9\n"
      "s_min_u32 s54, s54, .Lfd_end-.Lfd_pc-1024\n"
      "s_add_u32 s52, s52, s54\n"
      "s_addc_u32 s53, s53, 0\n"
      "s_and_b32 s52, s52, -16\n"
      "s_mov_b32 m0, 160896\n"
      "s_cmp_lg_u32 s36, s55\n"
      "s_cbranch_scc1 .Lfd_nowarm\n"
      "global_load_lds_dwordx4 v11, s[52:53]\n"
      ".Lfd_nowarm:\n"
      "v_lshrrev_b32 v1, 4, v0\n"
      "v_and_b32 v2, 15, v0\n"
      "s_lshr_b32 s56, s2, 4\n"
      "s_lshl_b32 s56, s56, 3\n"
      "s_and_b32 s57, s2, 7\n"
      "s_bfe_u32 s58, s2, 0x30004\n"
      "s_xor_b32 s58, s57, s58\n"
      "s_add_u32 s38, s56, s58\n"
      "s_lshr_b32 s39, s38, 2\n"
      "s_and_b32 s40, s38, 3\n"
      "v_lshlrev_b32 v5, 13, v1\n"
      "v_lshlrev_b32 v6, 1, v1\n"
      "v_xor_b32 v6, v2, v6\n"
      "v_lshl_add_u32 v26, v6, 4, v5\n"
      "s_mov_b32 s46, 110592\n"
      "s_mov_b32 s45, 110592\n"
      "s_cmp_lg_u32 s37, 0\n"
      "s_cmov_b32 s46, 89600\n"
      "s_cmov_b32 s45, 36864\n"
      "s_mul_i32 s58, s40, 0x120000\n"
      "s_lshl_b32 s56, s39, 8\n"
      "s_add_u32 s54, s58, s56\n"
      "s_lshl_b32 s56, s36, 15\n"
      "s_add_u32 s52, s54, s56\n"
      "s_mul_i32 s56, s36, 0x480\n"
      "s_add_u32 s53, s46, s56\n"
      "s_mov_b32 m0, s53\n"
      "s_waitcnt lgkmcnt(0)\n"
      "buffer_load_dwordx4 v26, s[4:7], s52 offen nt lds\n"
      "s_add_u32 s52, s52, 262144\n"
      "s_add_u32 m0, m0, 9216\n"
      "s_buffer_load_dword s97, s[12:15], 2048\n"
      "s_buffer_load_dword s96, s[8:11], 0\n"
      "buffer_load_dwordx4 v26, s[4:7], s52 offen nt lds\n"
      "s_cmp_eq_u32 s37, 0\n"
      "s_cbranch_scc1 .Lfd_roleA\n"
      "s_add_u32 s52, s52, 262144\n"
      "s_add_u32 m0, s53, 18432\n"
      "s_nop 0\n"
      "buffer_load_dwordx4 v26, s[4:7], s52 offen nt lds\n"
      "s_add_u32 s52, s52, 262144\n"
      "s_add_u32 m0, s53, 27648\n"
      "s_nop 0\n"
      "buffer_load_dwordx4 v26, s[4:7], s52 offen nt lds\n"
      "s_lshr_b32 s56, s36, 1\n"
      "s_add_u32 s56, 32, s56\n"
      "s_lshl_b32 s56, s56, 15\n"
      "s_add_u32 s56, s54, s56\n"
      "s_and_b32 s58, s36, 1\n"
      "s_lshl_b32 s58, s58, 14\n"
      "s_add_u32 s52, s56, s58\n"
      "s_lshr_b32 s58, s36, 1\n"
      "s_add_u32 s58, 32, s58\n"
      "s_mul_i32 s58, s58, 0x480\n"
      "s_add_u32 s58, s46, s58\n"
      "s_and_b32 s56, s36, 1\n"
      "s_lshl_b32 s56, s56, 9\n"
      "s_add_u32 m0, s58, s56\n"
      "s_and_b32 s56, s36, 1\n"
      "s_lshl_b32 s56, s56, 6\n"
      "v_xor_b32 v27, s56, v26\n"
      "s_mov_b32 exec_hi, 0\n"
      "buffer_load_dwordx4 v27, s[4:7], s52 offen nt lds\n"
      "s_mov_b32 exec_hi, -1\n"
      "s_cmp_lg_u32 s36, 2\n"
      "s_cbranch_scc1 .Lfd_x_1\n"
      "v_cmp_lt_u32 vcc, 17, v0\n"
      "v_mov_b32 v5, 864\n"
      "s_nop 0\n"
      "v_cndmask_b32 v5, 0, v5, vcc\n"
      "v_add_u32 v5, v5, v11\n"
      "s_mul_i32 s55, s40, 0x120\n"
      "s_mov_b32 m0, 160256\n"
      "s_mov_b32 exec_hi, 15\n"
      "buffer_load_dwordx4 v5, s[8:11], s55 offen lds\n"
      "s_mov_b32 exec_hi, -1\n"
      "buffer_load_dword v12, off, s[12:15], 0 sc1\n"
      ".Lfd_x_1:\n"
      "s_and_b32 s56, s2, 7\n"
      "s_lshl_b32 s56, s56, 4\n"
      "s_lshr_b32 s58, s2, 4\n"
      "s_add_u32 s41, s56, s58\n"
      "s_cmp_lg_u32 s36, 3\n"
      "s_cbranch_scc1 .Lfd_pt_2\n"
      "s_lshl_b32 s76, s40, 20\n"
      "s_buffer_load_dword s76, s[24:27], s76\n"
      "s_lshr_b32 s58, s41, 6\n"
      "s_lshl_b32 s58, s58, 23\n"
      "s_add_u32 s77, s58, 0\n"
      "s_buffer_load_dword s77, s[16:19], s77\n"
      "s_lshr_b32 s58, s41, 6\n"
      "s_lshl_b32 s58, s58, 23\n"
      "s_add_u32 s78, s58, 0x200000\n"
      "s_buffer_load_dword s78, s[16:19], s78\n"
      "s_lshr_b32 s58, s41, 6\n"
      "s_lshl_b32 s58, s58, 23\n"
      "s_add_u32 s79, s58, 0x400000\n"
      "s_buffer_load_dword s79, s[16:19], s79\n"
      "s_lshr_b32 s58, s41, 6\n"
      "s_lshl_b32 s58, s58, 23\n"
      "s_add_u32 s80, s58, 0x600000\n"
      "s_buffer_load_dword s80, s[16:19], s80\n"
      ".Lfd_pt_2:\n"
      "v_bfe_u32 v3, v0, 2, 2\n"
      "v_and_b32 v4, 3, v0\n"
      "s_mov_b32 s47, 0\n"
      "s_mov_b32 s48, 0xffff0000\n"
      "s_mov_b32 s49, 0xffff0000\n"
      "s_mov_b32 s50, 0\n"
      "s_mov_b32 s51, -1\n"
      "v_mul_u32_u24 v5, 0x480, v1\n"
      "v_add_u32 v5, s46, v5\n"
      "v_lshl_add_u32 v5, v3, 8, v5\n"
      "s_lshl_b32 s58, s36, 1\n"
      "v_lshrrev_b32 v6, 1, v4\n"
      "v_add_u32 v6, s58, v6\n"
      "v_lshlrev_b32 v7, 1, v3\n"
      "v_xor_b32 v7, v6, v7\n"
      "v_lshl_add_u32 v5, v7, 4, v5\n"
      "v_and_b32 v7, 1, v4\n"
      "v_lshl_add_u32 v28, v7, 3, v5\n"
      "v_and_b32 v5, 1, v0\n"
      "v_mul_u32_u24 v5, 0x120, v5\n"
      "v_add_u32 v5, 0x27200, v5\n"
      "v_lshl_add_u32 v29, v1, 3, v5\n"
      "s_lshl_b32 s58, s37, 7\n"
      "s_add_u32 s58, s58, s38\n"
      "s_lshl_b32 s58, s58, 10\n"
      "s_add_u32 s58, 0x1000, s58\n"
      "s_lshl_b32 s56, s36, 6\n"
      "s_add_u32 s56, s58, s56\n"
      "v_lshl_add_u32 v5, v1, 9, s56\n"
      "v_lshl_add_u32 v30, v2, 2, v5\n"
      "s_and_b32 s3, s41, 1\n"
      "s_and_b32 s44, s36, 3\n"
      "v_lshlrev_b32 v5, 16, v1\n"
      "v_lshlrev_b32 v7, 1, v1\n"
      "v_xor_b32 v7, v2, v7\n"
      "v_lshl_add_u32 v35, v7, 4, v5\n"
      "v_xor_b32 v36, 128, v35\n"
      "v_xor_b32 v37, 144, v35\n"
      "v_xor_b32 v38, 16, v35\n"
      "s_cmp_ge_u32 s36, 4\n"
      "s_cbranch_scc1 .Lfd_Bl0\n"
      "v_mov_b32 v6, 0\n"
      "v_mov_b32 v128, 0x24200\n"
      "ds_write_b32 v128, v6\n"
      "v_lshlrev_b32 v5, 15, v3\n"
      "v_lshl_add_u32 v7, v4, 2, v1\n"
      "v_lshl_add_u32 v121, v7, 4, v5\n"
      "s_lshl_b32 s56, s40, 7\n"
      "s_lshl_b32 s58, s36, 5\n"
      "s_add_u32 s58, s56, s58\n"
      "s_lshl_b32 s58, s58, 13\n"
      "s_lshl_b32 s56, s39, 8\n"
      "s_add_u32 s52, s58, s56\n"
      "s_lshl_b32 s56, s39, 10\n"
      "s_add_u32 s56, 0x81000, s56\n"
      "s_lshl_b32 s58, s40, 8\n"
      "s_add_u32 s58, s56, s58\n"
      "v_lshl_add_u32 v5, v1, 15, s58\n"
      "v_lshl_add_u32 v120, v2, 4, v5\n"
      "v_mov_b32 v124, -1\n"
      "v_mov_b32 v125, -1\n"
      "v_mov_b32 v126, -1\n"
      "v_mov_b32 v127, -1\n"
      "s_waitcnt vmcnt(4)\n"
      "buffer_load_dwordx4 v[40:43], v121, s[24:27], s52 offen nt\n"
      "s_add_u32 s52, s52, 8192\n"
      "buffer_load_dwordx4 v[44:47], v121, s[24:27], s52 offen nt\n"
      "s_add_u32 s52, s52, 8192\n"
      "s_waitcnt vmcnt(5)\n"
      "buffer_load_dwordx4 v[48:51], v121, s[24:27], s52 offen nt\n"
      "s_add_u32 s52, s52, 8192\n"
      "buffer_load_dwordx4 v[52:55], v121, s[24:27], s52 offen nt\n"
      "s_add_u32 s52, s52, 106496\n"
      "s_waitcnt vmcnt(6)\n"
      "buffer_load_dwordx4 v[56:59], v121, s[24:27], s52 offen nt\n"
      "s_add_u32 s52, s52, 8192\n"
      "buffer_load_dwordx4 v[60:63], v121, s[24:27], s52 offen nt\n"
      "s_add_u32 s52, s52, 8192\n"
      "s_waitcnt vmcnt(7)\n"
      "buffer_load_dwordx4 v[64:67], v121, s[24:27], s52 offen nt\n"
      "s_add_u32 s52, s52, 8192\n"
      "buffer_load_dwordx4 v[68:71], v121, s[24:27], s52 offen nt\n"
      "s_waitcnt vmcnt(8)\n"
      "s_cmp_lg_u32 s36, 0\n"
      "s_cbranch_scc1 .Lfd_Bs1\n"
      "s_mov_b32 exec_hi, 0\n"
      "buffer_store_dwordx4 v[124:127], v120, s[12:15], 0 offen sc1\n"
      "s_mov_b32 exec_hi, -1\n"
      "s_branch .Lfd_Bs1\n"
      ".Lfd_Bl0:\n"
      "s_lshl_b32 s65, s44, 10\n"
      "s_lshr_b32 s58, s44, 1\n"
      "s_xor_b32 s58, s44, s58\n"
      "s_and_b32 s58, s58, 1\n"
      "s_lshl_b32 s58, s58, 3\n"
      "s_lshr_b32 s56, s44, 1\n"
      "s_xor_b32 s56, s58, s56\n"
      "s_lshl_b32 s56, s56, 4\n"
      "v_xor_b32 v34, s56, v35\n"
      "s_waitcnt vmcnt(0)\n"
      ".Lfd_Bs1:\n"
      "v_mov_b32 v25, 162064\n"
      "s_cmp_lg_u32 s36, 2\n"
      "s_cbranch_scc1 .Lfd_e_3\n"
      "ds_write_b32 v25, v12\n"
      ".Lfd_e_3:\n"
      "s_waitcnt lgkmcnt(0)\n"
      "s_barrier\n"
      "ds_read_b32 v12, v25\n"
      "ds_read2_b64 v[72:75], v29 offset0:0 offset1:4\n"
      "ds_read_b64_tr_b16 v[90:91], v28 offset:0\n"
      "ds_read_b64_tr_b16 v[92:93], v28 offset:4608\n"
      "ds_read2_b64 v[76:79], v29 offset0:8 offset1:12\n"
      "ds_read_b64_tr_b16 v[94:95], v28 offset:9216\n"
      "ds_read_b64_tr_b16 v[96:97], v28 offset:13824\n"
      "ds_read2_b64 v[80:83], v29 offset0:16 offset1:20\n"
      "ds_read_b64_tr_b16 v[98:99], v28 offset:18432\n"
      "ds_read_b64_tr_b16 v[100:101], v28 offset:23040\n"
      "ds_read2_b64 v[84:87], v29 offset0:24 offset1:28\n"
      "ds_read_b64_tr_b16 v[102:103], v28 offset:27648\n"
      "ds_read_b64_tr_b16 v[104:105], v28 offset:32256\n"
      "ds_read_b64 v[88:89], v29 offset:256\n"
      "ds_read_b64_tr_b16 v[106:107], v28 offset:36864\n"
      "s_waitcnt lgkmcnt(11)\n"
      "v_mfma_f32_16x16x32_bf16 v[108:111], v[72:75], v[90:93], 0\n"
      "s_waitcnt lgkmcnt(8)\n"
      "v_mfma_f32_16x16x32_bf16 v[112:115], v[76:79], v[94:97], 0\n"
      "s_waitcnt lgkmcnt(5)\n"
      "v_mfma_f32_16x16x32_bf16 v[108:111], v[80:83], v[98:101], v[108:111]\n"
      "s_waitcnt lgkmcnt(2)\n"
      "v_mfma_f32_16x16x32_bf16 v[112:115], v[84:87], v[102:105], v[112:115]\n"
      "s_waitcnt lgkmcnt(0)\n"
      "v_mfma_f32_16x16x16_bf16 v[116:119], v[88:89], v[106:107], 0\n"
      "v_readfirstlane_b32 s43, v12\n"
      "s_add_u32 s43, s43, 1\n"
      "s_mov_b32 s42, s43\n"
      "v_mov_b32 v10, s43\n"
      "v_mov_b32 v33, s43\n"
      "s_nop 0\n"
      "v_pk_add_f32 v[108:109], v[108:109], v[112:113]\n"
      "s_nop 0\n"
      "v_pk_add_f32 v[108:109], v[108:109], v[116:117]\n"
      "v_cndmask_b32 v108, v108, v109, s[48:49]\n"
      "s_nop 1\n"
      "v_mov_b32_dpp v5, v108 quad_perm:[1,0,3,2] row_mask:0xf bank_mask:0xf\n"
      "v_cvt_pk_bf16_f32 v32, v108, v5\n"
      "s_mov_b32 exec_lo, 0x55555555\n"
      "s_mov_b32 exec_hi, 0\n"
      "buffer_store_dwordx2 v[32:33], v30, s[12:15], 0 offen sc1\n"
      "s_mov_b64 exec, -1\n"
      "s_lshl_b32 s56, s39, 7\n"
      "s_add_u32 s56, 0xf1000, s56\n"
      "s_lshl_b32 s58, s40, 2\n"
      "s_add_u32 s58, s56, s58\n"
      "v_mov_b32 v31, s58\n"
      "s_waitcnt lgkmcnt(0)\n"
      "s_barrier\n"
      "s_cmp_ge_u32 s36, 4\n"
      "s_cbranch_scc1 .Lfd_Bload\n"
      "s_cmp_lg_u32 s36, 3\n"
      "s_cbranch_scc1 .Lfd_pt_4\n"
      "s_lshl_b32 s84, s40, 20\n"
      "s_buffer_load_dword s84, s[20:23], s84\n"
      "s_mul_i32 s85, s38, 0x9000\n"
      "s_buffer_load_dword s85, s[28:31], s85\n"
      "s_mov_b32 s86, 0\n"
      "s_buffer_load_dword s86, s[32:35], s86\n"
      "s_mov_b32 s87, 0x2a4380\n"
      "s_buffer_load_dword s87, s[12:15], s87\n"
      "s_mov_b32 s88, 0x4ac3fc\n"
      "s_buffer_load_dword s88, s[12:15], s88\n"
      ".Lfd_pt_4:\n"
      "s_cmp_lg_u32 s36, 1\n"
      "s_cbranch_scc1 .Lfd_Bnh\n"
      "s_mov_b32 exec_lo, 1\n"
      "s_mov_b32 exec_hi, 0\n"
      "buffer_store_dword v10, v31, s[12:15], 0 offen sc1\n"
      "s_mov_b64 exec, -1\n"
      ".Lfd_Bnh:\n"
      "s_lshl_b32 s58, s39, 2\n"
      "s_add_u32 s58, 0x80, s58\n"
      "s_lshl_b32 s58, s58, 10\n"
      "s_add_u32 s58, 0x1000, s58\n"
      "v_lshl_add_u32 v107, v0, 4, s58\n"
      "s_mov_b32 s53, 4096\n"
      "s_mov_b32 s55, 0x3e0293ee\n"
      "v_mov_b32 v111, 1\n"
      "s_cmp_lg_u32 s36, 0\n"
      "s_cbranch_scc1 .Lfd_Bq0\n"
      "s_lshl_b32 s58, s39, 7\n"
      "s_add_u32 s52, 0xf1000, s58\n"
      "s_mov_b32 s61, 0\n"
      ".Lfd_p_5:\n"
      "s_buffer_load_dwordx4 s[68:71], s[12:15], s52 glc\n"
      "s_waitcnt lgkmcnt(0)\n"
      "s_cmp_eq_u64 s[68:69], s[42:43]\n"
      "s_cbranch_scc0 .Lfd_r_6\n"
      "s_cmp_eq_u64 s[70:71], s[42:43]\n"
      "s_cbranch_scc0 .Lfd_r_6\n"
      ".Lfd_d_7:\n"
      "buffer_load_dwordx4 v[72:75], v107, s[12:15], 0 offen sc1\n"
      "buffer_load_dwordx4 v[76:79], v107, s[12:15], 0 offen offset:1024 sc1\n"
      "buffer_load_dwordx4 v[80:83], v107, s[12:15], 0 offen offset:2048 sc1\n"
      "buffer_load_dwordx4 v[84:87], v107, s[12:15], 0 offen offset:3072 sc1\n"
      ".Lfd_Bq0:\n"
      "s_lshl_b32 s58, s39, 10\n"
      "s_add_u32 s58, 0x81000, s58\n"
      "s_lshl_b32 s56, s40, 8\n"
      "s_add_u32 s56, s58, s56\n"
      "s_lshl_b32 s58, s36, 6\n"
      "s_add_u32 s58, s56, s58\n"
      "v_and_b32 v5, 1, v3\n"
      "v_lshl_add_u32 v5, v5, 15, s58\n"
      "v_lshrrev_b32 v7, 1, v3\n"
      "v_lshl_add_u32 v5, v7, 5, v5\n"
      "v_lshl_add_u32 v104, v1, 3, v5\n"
      "v_and_b32 v5, 1, v3\n"
      "v_lshlrev_b32 v5, 8, v5\n"
      "v_add_u32 v5, 0x24000, v5\n"
      "v_lshl_add_u32 v5, v4, 6, v5\n"
      "v_lshl_add_u32 v105, v1, 4, v5\n"
      "v_lshlrev_b32 v5, 3, v0\n"
      "v_add_u32 v106, 0x24000, v5\n"
      "v_mov_b32 v120, 0\n"
      "v_mov_b32 v121, 0\n"
      "v_mov_b32 v122, 0\n"
      "v_mov_b32 v123, 0\n"
      "v_mov_b32 v124, 0\n"
      "v_mov_b32 v125, 0\n"
      "v_mov_b32 v126, 0\n"
      "v_mov_b32 v127, 0\n"
      "s_lshr_b32 s58, s38, 4\n"
      "s_lshl_b32 s58, s58, 13\n"
      "s_add_u32 s58, 0x91000, s58\n"
      "s_bfe_u32 s56, s38, 0x20002\n"
      "s_lshl_b32 s56, s56, 4\n"
      "s_add_u32 s56, s58, s56\n"
      "s_and_b32 s58, s38, 3\n"
      "s_lshl_b32 s58, s58, 2\n"
      "s_add_u32 s58, s56, s58\n"
      "v_lshl_add_u32 v108, v0, 8, s58\n"
      "s_lshr_b32 s58, s41, 6\n"
      "s_lshl_b32 s58, s58, 15\n"
      "s_add_u32 s58, 0x81000, s58\n"
      "s_lshl_b32 s56, s3, 4\n"
      "s_lshl_b32 s57, s36, 2\n"
      "s_add_u32 s57, s56, s57\n"
      "s_lshl_b32 s57, s57, 10\n"
      "s_add_u32 s57, s58, s57\n"
      "v_lshl_add_u32 v109, v0, 4, s57\n"
      "s_lshl_b32 s57, s36, 10\n"
      "s_add_u32 s57, 0x20000, s57\n"
      "v_lshl_add_u32 v5, v1, 12, s57\n"
      "s_lshr_b32 s57, s36, 1\n"
      "s_xor_b32 s57, s36, s57\n"
      "s_and_b32 s57, s57, 1\n"
      "s_lshl_b32 s57, s57, 3\n"
      "v_xor_b32 v7, s57, v2\n"
      "s_lshr_b32 s57, s36, 1\n"
      "v_xor_b32 v7, s57, v7\n"
      "v_lshl_add_u32 v110, v7, 4, v5\n"
      "s_call_b64 s[0:1], .Lfd_sub_b\n"
      "s_call_b64 s[0:1], .Lfd_sub_t\n"
      ".Lfd_Bq:\n"
      "s_waitcnt vmcnt(0)\n"
      "s_cmp_lg_u32 s36, 0\n"
      "s_cbranch_scc1 .Lfd_Bqw\n"
      "v_bitop3_b32 v5, v73, v75, s43 bitop3:0x7e\n"
      "v_bitop3_b32 v7, v77, v79, s43 bitop3:0x7e\n"
      "v_or_b32 v5, v5, v7\n"
      "v_bitop3_b32 v6, v81, v83, s43 bitop3:0x7e\n"
      "v_bitop3_b32 v7, v85, v87, s43 bitop3:0x7e\n"
      "v_or3_b32 v5, v5, v6, v7\n"
      "v_cmp_ne_u32 vcc, 0, v5\n"
      "s_cbranch_vccnz .Lfd_r_8\n"
      ".Lfd_d_9:\n"
      "v_lshlrev_b32 v88, 16, v72\n"
      "v_and_b32 v89, 0xffff0000, v72\n"
      "v_lshlrev_b32 v90, 16, v74\n"
      "v_and_b32 v91, 0xffff0000, v74\n"
      "v_lshlrev_b32 v92, 16, v76\n"
      "v_and_b32 v93, 0xffff0000, v76\n"
      "v_lshlrev_b32 v94, 16, v78\n"
      "v_and_b32 v95, 0xffff0000, v78\n"
      "v_pk_add_f32 v[88:89], v[88:89], v[92:93]\n"
      "v_pk_add_f32 v[90:91], v[90:91], v[94:95]\n"
      "v_lshlrev_b32 v92, 16, v80\n"
      "v_and_b32 v93, 0xffff0000, v80\n"
      "v_lshlrev_b32 v94, 16, v82\n"
      "v_and_b32 v95, 0xffff0000, v82\n"
      "v_pk_add_f32 v[88:89], v[88:89], v[92:93]\n"
      "v_pk_add_f32 v[90:91], v[90:91], v[94:95]\n"
      "v_lshlrev_b32 v92, 16, v84\n"
      "v_and_b32 v93, 0xffff0000, v84\n"
      "v_lshlrev_b32 v94, 16, v86\n"
      "v_and_b32 v95, 0xffff0000, v86\n"
      "v_pk_add_f32 v[88:89], v[88:89], v[92:93]\n"
      "v_pk_add_f32 v[90:91], v[90:91], v[94:95]\n"
      "v_cvt_pk_bf16_f32 v88, v88, v89\n"
      "v_cvt_pk_bf16_f32 v89, v90, v91\n"
      "ds_write_b64 v106, v[88:89]\n"
      "ds_write_b32 v128, v111\n"
      "s_branch .Lfd_Bqr\n"
      ".Lfd_r_6:\n"
      "s_add_u32 s61, s61, 1\n"
      "s_cmp_lt_u32 s61, 4096\n"
      "s_cbranch_scc1 .Lfd_p_5\n"
      "s_branch .Lfd_d_7\n"
      ".Lfd_r_8:\n"
      "s_add_u32 s47, s47, 1\n"
      "s_cmp_lt_u32 s47, 0x10000\n"
      "s_cbranch_scc0 .Lfd_d_9\n"
      "buffer_load_dwordx4 v[72:75], v107, s[12:15], 0 offen offset:0 sc1\n"
      "buffer_load_dwordx4 v[76:79], v107, s[12:15], 0 offen offset:1024 sc1\n"
      "buffer_load_dwordx4 v[80:83], v107, s[12:15], 0 offen offset:2048 sc1\n"
      "buffer_load_dwordx4 v[84:87], v107, s[12:15], 0 offen offset:3072 sc1\n"
      "s_branch .Lfd_Bq\n"
      ".Lfd_Bqw:\n"
      ".Lfd_s_10:\n"
      "ds_read_b32 v5, v128 offset:0\n"
      "s_waitcnt lgkmcnt(0)\n"
      "v_cmp_ne_u32 vcc, 1, v5\n"
      "s_cbranch_vccnz .Lfd_s_10\n"
      ".Lfd_Bqr:\n"
      "s_mov_b32 exec_lo, 0xff00ff\n"
      "s_mov_b32 exec_hi, 0xff00ff\n"
      "ds_read_b128 v[120:123], v105\n"
      "s_mov_b32 exec_lo, 0xff00ff00\n"
      "s_mov_b32 exec_hi, 0xff00ff00\n"
      "ds_read_b128 v[124:127], v105\n"
      "s_mov_b64 exec, -1\n"
      "s_waitcnt lgkmcnt(0)\n"
      "v_mfma_f32_16x16x32_bf16 v[72:75], v[40:43], v[120:123], 0\n"
      "v_mfma_f32_16x16x32_bf16 v[76:79], v[44:47], v[120:123], 0\n"
      "v_mfma_f32_16x16x32_bf16 v[80:83], v[48:51], v[120:123], 0\n"
      "v_mfma_f32_16x16x32_bf16 v[84:87], v[52:55], v[120:123], 0\n"
      "v_mfma_f32_16x16x32_bf16 v[72:75], v[56:59], v[124:127], v[72:75]\n"
      "v_mfma_f32_16x16x32_bf16 v[76:79], v[60:63], v[124:127], v[76:79]\n"
      "v_mfma_f32_16x16x32_bf16 v[80:83], v[64:67], v[124:127], v[80:83]\n"
      "v_mfma_f32_16x16x32_bf16 v[84:87], v[68:71], v[124:127], v[84:87]\n"
      "s_nop 4\n"
      "v_add_f32_dpp v72, v73, v72 quad_perm:[1,1,1,1] row_mask:0xf bank_mask:0xf\n"
      "v_add_f32_dpp v76, v77, v76 quad_perm:[1,1,1,1] row_mask:0xf bank_mask:0xf\n"
      "v_add_f32_dpp v80, v81, v80 quad_perm:[1,1,1,1] row_mask:0xf bank_mask:0xf\n"
      "v_add_f32_dpp v84, v85, v84 quad_perm:[1,1,1,1] row_mask:0xf bank_mask:0xf\n"
      "v_add_f32_dpp v72, v74, v72 quad_perm:[2,2,2,2] row_mask:0xf bank_mask:0xf\n"
      "v_add_f32_dpp v76, v78, v76 quad_perm:[2,2,2,2] row_mask:0xf bank_mask:0xf\n"
      "v_add_f32_dpp v80, v82, v80 quad_perm:[2,2,2,2] row_mask:0xf bank_mask:0xf\n"
      "v_add_f32_dpp v84, v86, v84 quad_perm:[2,2,2,2] row_mask:0xf bank_mask:0xf\n"
      "v_add_f32_dpp v72, v75, v72 quad_perm:[3,3,3,3] row_mask:0xf bank_mask:0xf\n"
      "v_add_f32_dpp v76, v79, v76 quad_perm:[3,3,3,3] row_mask:0xf bank_mask:0xf\n"
      "v_add_f32_dpp v80, v83, v80 quad_perm:[3,3,3,3] row_mask:0xf bank_mask:0xf\n"
      "v_add_f32_dpp v84, v87, v84 quad_perm:[3,3,3,3] row_mask:0xf bank_mask:0xf\n"
      "v_mul_f32 v72, s55, v72\n"
      "v_mul_f32 v76, s55, v76\n"
      "v_mul_f32 v80, s55, v80\n"
      "v_mul_f32 v84, s55, v84\n"
      "v_cvt_pk_bf16_f32 v74, v72, v76\n"
      "v_cvt_pk_bf16_f32 v75, v80, v84\n"
      "s_mov_b32 exec_lo, 0x11111111\n"
      "s_mov_b32 exec_hi, 0x11111111\n"
      "buffer_store_dwordx2 v[74:75], v104, s[12:15], 0 offen sc1\n"
      "s_mov_b64 exec, -1\n"
      "s_cmp_lg_u32 s36, 0\n"
      "s_cbranch_scc1 .Lfd_Bnf\n"
      "s_mov_b32 exec_lo, 0xff\n"
      "s_mov_b32 exec_hi, 0\n"
      "buffer_store_dword v10, v108, s[12:15], 0 offen sc1\n"
      "s_mov_b64 exec, -1\n"
      ".Lfd_Bnf:\n"
      "s_lshl_b32 s57, s3, 2\n"
      "s_add_u32 s57, s57, s36\n"
      "s_lshl_b32 s57, s57, 13\n"
      "s_add_u32 s57, 0x91000, s57\n"
      "s_bfe_u32 s58, s41, 0x30001\n"
      "s_lshl_b32 s58, s58, 8\n"
      "s_add_u32 s52, s57, s58\n"
      "v_xor_b32 v88, 288, v110\n"
      "v_xor_b32 v89, 576, v110\n"
      "v_xor_b32 v90, 864, v110\n"
      "s_branch .Lfd_BL0\n"
      ".Lfd_Bload:\n"
      "s_lshr_b32 s58, s41, 6\n"
      "s_lshl_b32 s58, s58, 23\n"
      "s_and_b32 s57, s41, 63\n"
      "s_lshl_b32 s57, s57, 10\n"
      "s_add_u32 s57, s58, s57\n"
      "s_lshl_b32 s58, s44, 18\n"
      "s_add_u32 s64, s57, s58\n"
      "s_add_u32 m0, s65, 0\n"
      "s_nop 0\n"
      "buffer_load_dwordx4 v34, s[16:19], s64 offen lds\n"
      "s_add_u32 m0, s65, 32512\n"
      "s_nop 0\n"
      "buffer_load_dwordx4 v34, s[16:19], s64 offen offset:256 lds\n"
      "s_add_u32 m0, s65, 65024\n"
      "s_nop 0\n"
      "buffer_load_dwordx4 v34, s[16:19], s64 offen offset:512 lds\n"
      "s_add_u32 m0, s65, 97536\n"
      "s_nop 0\n"
      "buffer_load_dwordx4 v34, s[16:19], s64 offen offset:768 lds\n"
      "s_add_u32 s64, s64, 1048576\n"
      "s_add_u32 m0, s65, 4096\n"
      "s_nop 0\n"
      "buffer_load_dwordx4 v34, s[16:19], s64 offen lds\n"
      "s_add_u32 m0, s65, 36608\n"
      "s_nop 0\n"
      "buffer_load_dwordx4 v34, s[16:19], s64 offen offset:256 lds\n"
      "s_add_u32 m0, s65, 69120\n"
      "s_nop 0\n"
      "buffer_load_dwordx4 v34, s[16:19], s64 offen offset:512 lds\n"
      "s_call_b64 s[0:1], .Lfd_sub_b\n"
      "s_call_b64 s[0:1], .Lfd_sub_t\n"
      "s_add_u32 m0, s65, 101632\n"
      "s_waitcnt vmcnt(7)\n"
      "buffer_load_dwordx4 v34, s[16:19], s64 offen offset:768 lds\n"
      "s_add_u32 s64, s64, 1048576\n"
      "s_add_u32 m0, s65, 8192\n"
      "s_waitcnt vmcnt(7)\n"
      "buffer_load_dwordx4 v34, s[16:19], s64 offen lds\n"
      "s_add_u32 m0, s65, 40704\n"
      "s_waitcnt vmcnt(7)\n"
      "buffer_load_dwordx4 v34, s[16:19], s64 offen offset:256 lds\n"
      "s_add_u32 m0, s65, 73216\n"
      "s_waitcnt vmcnt(7)\n"
      "buffer_load_dwordx4 v34, s[16:19], s64 offen offset:512 lds\n"
      "s_add_u32 m0, s65, 105728\n"
      "s_waitcnt vmcnt(7)\n"
      "buffer_load_dwordx4 v34, s[16:19], s64 offen offset:768 lds\n"
      "s_add_u32 s64, s64, 1048576\n"
      "s_add_u32 m0, s65, 12288\n"
      "s_waitcnt vmcnt(7)\n"
      "buffer_load_dwordx4 v34, s[16:19], s64 offen lds\n"
      "s_add_u32 m0, s65, 44800\n"
      "s_waitcnt vmcnt(7)\n"
      "buffer_load_dwordx4 v34, s[16:19], s64 offen offset:256 lds\n"
      "s_add_u32 m0, s65, 77312\n"
      "s_waitcnt vmcnt(7)\n"
      "buffer_load_dwordx4 v34, s[16:19], s64 offen offset:512 lds\n"
      "s_add_u32 m0, s65, 109824\n"
      "s_waitcnt vmcnt(7)\n"
      "buffer_load_dwordx4 v34, s[16:19], s64 offen offset:768 lds\n"
      "s_add_u32 s64, s64, 1048576\n"
      "s_add_u32 m0, s65, 16384\n"
      "s_waitcnt vmcnt(7)\n"
      "buffer_load_dwordx4 v34, s[16:19], s64 offen lds\n"
      "s_add_u32 m0, s65, 48896\n"
      "s_waitcnt vmcnt(7)\n"
      "buffer_load_dwordx4 v34, s[16:19], s64 offen offset:256 lds\n"
      "s_add_u32 m0, s65, 81408\n"
      "s_waitcnt vmcnt(7)\n"
      "buffer_load_dwordx4 v34, s[16:19], s64 offen offset:512 lds\n"
      "s_add_u32 m0, s65, 113920\n"
      "s_waitcnt vmcnt(7)\n"
      "buffer_load_dwordx4 v34, s[16:19], s64 offen offset:768 lds\n"
      "s_add_u32 s64, s64, 1048576\n"
      "s_add_u32 m0, s65, 20480\n"
      "s_waitcnt vmcnt(7)\n"
      "buffer_load_dwordx4 v34, s[16:19], s64 offen lds\n"
      "s_add_u32 m0, s65, 52992\n"
      "s_waitcnt vmcnt(7)\n"
      "buffer_load_dwordx4 v34, s[16:19], s64 offen offset:256 lds\n"
      "s_add_u32 m0, s65, 85504\n"
      "s_waitcnt vmcnt(7)\n"
      "buffer_load_dwordx4 v34, s[16:19], s64 offen offset:512 lds\n"
      "s_add_u32 m0, s65, 118016\n"
      "s_waitcnt vmcnt(7)\n"
      "buffer_load_dwordx4 v34, s[16:19], s64 offen offset:768 lds\n"
      "s_add_u32 s64, s64, 1048576\n"
      "s_add_u32 m0, s65, 24576\n"
      "s_waitcnt vmcnt(7)\n"
      "buffer_load_dwordx4 v34, s[16:19], s64 offen lds\n"
      "s_add_u32 m0, s65, 57088\n"
      "s_waitcnt vmcnt(7)\n"
      "buffer_load_dwordx4 v34, s[16:19], s64 offen offset:256 lds\n"
      "s_add_u32 m0, s65, 89600\n"
      "s_waitcnt vmcnt(7)\n"
      "buffer_load_dwordx4 v34, s[16:19], s64 offen offset:512 lds\n"
      "s_add_u32 m0, s65, 122112\n"
      "s_waitcnt vmcnt(7)\n"
      "buffer_load_dwordx4 v34, s[16:19], s64 offen offset:768 lds\n"
      "s_add_u32 s64, s64, 1048576\n"
      "s_add_u32 m0, s65, 28672\n"
      "s_waitcnt vmcnt(7)\n"
      "buffer_load_dwordx4 v34, s[16:19], s64 offen lds\n"
      "s_add_u32 m0, s65, 61184\n"
      "s_waitcnt vmcnt(7)\n"
      "buffer_load_dwordx4 v34, s[16:19], s64 offen offset:256 lds\n"
      "s_add_u32 m0, s65, 93696\n"
      "s_waitcnt vmcnt(7)\n"
      "buffer_load_dwordx4 v34, s[16:19], s64 offen offset:512 lds\n"
      "s_add_u32 m0, s65, 126208\n"
      "s_waitcnt vmcnt(7)\n"
      "buffer_load_dwordx4 v34, s[16:19], s64 offen offset:768 lds\n"
      ".Lfd_BL0:\n"
      "s_xor_b32 s58, s41, 1\n"
      "s_lshr_b32 s58, s58, 6\n"
      "s_lshl_b32 s58, s58, 23\n"
      "s_xor_b32 s57, s41, 1\n"
      "s_and_b32 s57, s57, 63\n"
      "s_lshl_b32 s57, s57, 10\n"
      "s_add_u32 s57, s58, s57\n"
      "s_xor_b32 s58, s36, 0\n"
      "s_lshl_b32 s58, s58, 20\n"
      "s_add_u32 s84, s57, s58\n"
      "s_add_u32 s85, s84, 262144\n"
      "s_add_u32 s86, s84, 524288\n"
      "s_add_u32 s87, s84, 786432\n"
      "s_lshl_b32 s65, s36, 12\n"
      "s_lshl_b32 s58, s40, 7\n"
      "s_lshr_b32 s57, s36, 2\n"
      "s_lshl_b32 s57, s57, 6\n"
      "s_add_u32 s57, s58, s57\n"
      "s_and_b32 s58, s36, 3\n"
      "s_lshl_b32 s58, s58, 2\n"
      "s_add_u32 s58, s57, s58\n"
      "s_lshl_b32 s58, s58, 13\n"
      "s_lshl_b32 s57, s39, 8\n"
      "s_add_u32 s54, s58, s57\n"
      "s_barrier\n"
      "s_cmp_ge_u32 s36, 4\n"
      "s_cbranch_scc1 .Lfd_Bm1\n"
      "s_mov_b32 s61, 0\n"
      "s_buffer_load_dwordx16 s[68:83], s[12:15], s52 glc\n"
      "s_call_b64 s[0:1], .Lfd_sub_cf\n"
      "s_call_b64 s[0:1], .Lfd_sub_a1\n"
      "s_branch .Lfd_c_14\n"
      ".Lfd_p_11:\n"
      "s_buffer_load_dwordx16 s[68:83], s[12:15], s52 glc\n"
      ".Lfd_c_14:\n"
      "s_waitcnt lgkmcnt(0)\n"
      "s_cmp_eq_u64 s[68:69], s[42:43]\n"
      "s_cbranch_scc0 .Lfd_r_12\n"
      "s_cmp_eq_u64 s[70:71], s[42:43]\n"
      "s_cbranch_scc0 .Lfd_r_12\n"
      "s_cmp_eq_u64 s[72:73], s[42:43]\n"
      "s_cbranch_scc0 .Lfd_r_12\n"
      "s_cmp_eq_u64 s[74:75], s[42:43]\n"
      "s_cbranch_scc0 .Lfd_r_12\n"
      "s_cmp_eq_u64 s[76:77], s[42:43]\n"
      "s_cbranch_scc0 .Lfd_r_12\n"
      "s_cmp_eq_u64 s[78:79], s[42:43]\n"
      "s_cbranch_scc0 .Lfd_r_12\n"
      "s_cmp_eq_u64 s[80:81], s[42:43]\n"
      "s_cbranch_scc0 .Lfd_r_12\n"
      "s_cmp_eq_u64 s[82:83], s[42:43]\n"
      "s_cbranch_scc0 .Lfd_r_12\n"
      ".Lfd_d_13:\n"
      "buffer_load_dwordx4 v[72:75], v109, s[12:15], 0 offen sc1\n"
      "buffer_load_dwordx4 v[76:79], v109, s[12:15], 0 offen offset:1024 sc1\n"
      "buffer_load_dwordx4 v[80:83], v109, s[12:15], 0 offen offset:2048 sc1\n"
      "buffer_load_dwordx4 v[84:87], v109, s[12:15], 0 offen offset:3072 sc1\n"
      "s_barrier\n"
      "s_add_u32 m0, s65, 65024\n"
      "s_nop 0\n"
      "buffer_load_dwordx4 v35, s[16:19], s84 offen offset:512 lds\n"
      "s_add_u32 m0, s65, 66048\n"
      "s_nop 0\n"
      "buffer_load_dwordx4 v36, s[16:19], s85 offen offset:512 lds\n"
      "s_add_u32 m0, s65, 67072\n"
      "s_nop 0\n"
      "buffer_load_dwordx4 v37, s[16:19], s86 offen offset:512 lds\n"
      "s_add_u32 m0, s65, 68096\n"
      "s_nop 0\n"
      "buffer_load_dwordx4 v38, s[16:19], s87 offen offset:512 lds\n"
      "s_barrier\n"
      "ds_read_b64_tr_b16 v[132:133], v248 offset:24576\n"
      "ds_read_b64_tr_b16 v[148:149], v249 offset:24576\n"
      "ds_read_b64_tr_b16 v[164:165], v250 offset:24576\n"
      "ds_read_b64_tr_b16 v[180:181], v251 offset:24576\n"
      "ds_read_b64_tr_b16 v[134:135], v248 offset:28672\n"
      "ds_read_b64_tr_b16 v[150:151], v249 offset:28672\n"
      "ds_read_b64_tr_b16 v[166:167], v250 offset:28672\n"
      "ds_read_b64_tr_b16 v[182:183], v251 offset:28672\n"
      ".Lfd_Bqa:\n"
      "s_waitcnt vmcnt(7)\n"
      "v_max3_u32 v5, v73, v75, 0\n"
      "s_waitcnt vmcnt(6)\n"
      "v_max3_u32 v5, v77, v79, v5\n"
      "s_waitcnt vmcnt(5)\n"
      "v_max3_u32 v5, v81, v83, v5\n"
      "s_waitcnt vmcnt(4)\n"
      "v_max3_u32 v5, v85, v87, v5\n"
      "v_cmp_eq_u32 vcc, -1, v5\n"
      "s_cbranch_vccnz .Lfd_r_16\n"
      ".Lfd_d_17:\n"
      "ds_write_b128 v110, v[72:75]\n"
      "ds_write_b128 v88, v[76:79]\n"
      "ds_write_b128 v89, v[80:83]\n"
      "ds_write_b128 v90, v[84:87]\n"
      "s_add_u32 m0, s65, 97536\n"
      "s_nop 0\n"
      "buffer_load_dwordx4 v35, s[16:19], s84 offen offset:768 lds\n"
      "s_add_u32 m0, s65, 98560\n"
      "s_nop 0\n"
      "buffer_load_dwordx4 v36, s[16:19], s85 offen offset:768 lds\n"
      "s_add_u32 m0, s65, 99584\n"
      "s_nop 0\n"
      "buffer_load_dwordx4 v37, s[16:19], s86 offen offset:768 lds\n"
      "s_add_u32 m0, s65, 100608\n"
      "s_nop 0\n"
      "buffer_load_dwordx4 v38, s[16:19], s87 offen offset:768 lds\n"
      "s_waitcnt lgkmcnt(0)\n"
      "s_barrier\n"
      "s_branch .Lfd_BX\n"
      ".Lfd_r_12:\n"
      "s_sleep 2\n"
      "s_add_u32 s61, s61, 1\n"
      "s_cmp_lt_u32 s61, 4096\n"
      "s_cbranch_scc1 .Lfd_p_11\n"
      "v_and_b32 v5, 15, v0\n"
      "v_lshlrev_b32 v5, 2, v5\n"
      "v_bfe_u32 v6, v0, 4, 0\n"
      "v_lshl_add_u32 v5, v6, 6, v5\n"
      ".Lfd_v_15:\n"
      "buffer_load_dword v6, v5, s[12:15], s52 offen sc1\n"
      "s_waitcnt vmcnt(0)\n"
      "v_cmp_ne_u32 vcc, s43, v6\n"
      "s_cbranch_vccz .Lfd_d_13\n"
      "s_add_u32 s47, s47, 1\n"
      "s_cmp_lt_u32 s47, 0x10000\n"
      "s_cbranch_scc1 .Lfd_v_15\n"
      "s_branch .Lfd_d_13\n"
      ".Lfd_r_16:\n"
      "s_add_u32 s47, s47, 1\n"
      "s_cmp_lt_u32 s47, 0x10000\n"
      "s_cbranch_scc0 .Lfd_d_17\n"
      "s_sleep 4\n"
      "buffer_load_dwordx4 v[72:75], v109, s[12:15], 0 offen offset:0 sc1\n"
      "buffer_load_dwordx4 v[76:79], v109, s[12:15], 0 offen offset:1024 sc1\n"
      "buffer_load_dwordx4 v[80:83], v109, s[12:15], 0 offen offset:2048 sc1\n"
      "buffer_load_dwordx4 v[84:87], v109, s[12:15], 0 offen offset:3072 sc1\n"
      "s_waitcnt vmcnt(0)\n"
      "s_branch .Lfd_Bqa\n"
      ".Lfd_Bm1:\n"
      "s_call_b64 s[0:1], .Lfd_sub_cf\n"
      "s_cmp_ge_u32 s36, 6\n"
      "s_cbranch_scc1 .Lfd_Bm2\n"
      "s_call_b64 s[0:1], .Lfd_sub_a1\n"
      "s_waitcnt lgkmcnt(0)\n"
      "s_barrier\n"
      "s_add_u32 m0, s65, 65024\n"
      "s_nop 0\n"
      "buffer_load_dwordx4 v35, s[16:19], s84 offen offset:512 lds\n"
      "s_add_u32 m0, s65, 66048\n"
      "s_nop 0\n"
      "buffer_load_dwordx4 v36, s[16:19], s85 offen offset:512 lds\n"
      "s_add_u32 m0, s65, 67072\n"
      "s_nop 0\n"
      "buffer_load_dwordx4 v37, s[16:19], s86 offen offset:512 lds\n"
      "s_add_u32 m0, s65, 68096\n"
      "s_nop 0\n"
      "buffer_load_dwordx4 v38, s[16:19], s87 offen offset:512 lds\n"
      "s_waitcnt vmcnt(4)\n"
      "s_barrier\n"
      "ds_read_b64_tr_b16 v[132:133], v248 offset:24576\n"
      "ds_read_b64_tr_b16 v[148:149], v249 offset:24576\n"
      "ds_read_b64_tr_b16 v[164:165], v250 offset:24576\n"
      "ds_read_b64_tr_b16 v[180:181], v251 offset:24576\n"
      "ds_read_b64_tr_b16 v[134:135], v248 offset:28672\n"
      "ds_read_b64_tr_b16 v[150:151], v249 offset:28672\n"
      "ds_read_b64_tr_b16 v[166:167], v250 offset:28672\n"
      "ds_read_b64_tr_b16 v[182:183], v251 offset:28672\n"
      "s_add_u32 m0, s65, 97536\n"
      "s_nop 0\n"
      "buffer_load_dwordx4 v35, s[16:19], s84 offen offset:768 lds\n"
      "s_add_u32 m0, s65, 98560\n"
      "s_nop 0\n"
      "buffer_load_dwordx4 v36, s[16:19], s85 offen offset:768 lds\n"
      "s_add_u32 m0, s65, 99584\n"
      "s_nop 0\n"
      "buffer_load_dwordx4 v37, s[16:19], s86 offen offset:768 lds\n"
      "s_add_u32 m0, s65, 100608\n"
      "s_nop 0\n"
      "buffer_load_dwordx4 v38, s[16:19], s87 offen offset:768 lds\n"
      "s_waitcnt lgkmcnt(0)\n"
      "s_barrier\n"
      "s_branch .Lfd_BX\n"
      ".Lfd_Bm2:\n"
      "s_waitcnt lgkmcnt(0)\n"
      "s_barrier\n"
      "s_waitcnt vmcnt(0)\n"
      "s_barrier\n"
      "ds_read_b64_tr_b16 v[132:133], v248 offset:24576\n"
      "ds_read_b64_tr_b16 v[148:149], v249 offset:24576\n"
      "ds_read_b64_tr_b16 v[164:165], v250 offset:24576\n"
      "ds_read_b64_tr_b16 v[180:181], v251 offset:24576\n"
      "ds_read_b64_tr_b16 v[134:135], v248 offset:28672\n"
      "ds_read_b64_tr_b16 v[150:151], v249 offset:28672\n"
      "ds_read_b64_tr_b16 v[166:167], v250 offset:28672\n"
      "ds_read_b64_tr_b16 v[182:183], v251 offset:28672\n"
      "s_call_b64 s[0:1], .Lfd_sub_a1\n"
      "s_setprio 1\n"
      "s_waitcnt lgkmcnt(0)\n"
      "s_barrier\n"
      "s_add_u32 m0, s65, 65024\n"
      "s_nop 0\n"
      "buffer_load_dwordx4 v35, s[16:19], s84 offen offset:512 lds\n"
      "s_add_u32 m0, s65, 66048\n"
      "s_nop 0\n"
      "buffer_load_dwordx4 v36, s[16:19], s85 offen offset:512 lds\n"
      "s_add_u32 m0, s65, 67072\n"
      "s_nop 0\n"
      "buffer_load_dwordx4 v37, s[16:19], s86 offen offset:512 lds\n"
      "s_add_u32 m0, s65, 68096\n"
      "s_nop 0\n"
      "buffer_load_dwordx4 v38, s[16:19], s87 offen offset:512 lds\n"
      "s_add_u32 m0, s65, 97536\n"
      "s_nop 0\n"
      "buffer_load_dwordx4 v35, s[16:19], s84 offen offset:768 lds\n"
      "s_add_u32 m0, s65, 98560\n"
      "s_nop 0\n"
      "buffer_load_dwordx4 v36, s[16:19], s85 offen offset:768 lds\n"
      "s_add_u32 m0, s65, 99584\n"
      "s_nop 0\n"
      "buffer_load_dwordx4 v37, s[16:19], s86 offen offset:768 lds\n"
      "s_add_u32 m0, s65, 100608\n"
      "s_nop 0\n"
      "buffer_load_dwordx4 v38, s[16:19], s87 offen offset:768 lds\n"
      ".Lfd_BX:\n"
      "ds_read_b128 v[40:43], v116 offset:8192\n"
      "ds_read_b128 v[44:47], v117 offset:8192\n"
      "ds_read_b128 v[48:51], v118 offset:8192\n"
      "ds_read_b128 v[52:55], v119 offset:8192\n"
      "s_waitcnt lgkmcnt(3)\n"
      "v_mfma_f32_16x16x32_bf16 v[104:107], v[216:219], v[40:43], 0\n"
      "s_waitcnt lgkmcnt(2)\n"
      "v_mfma_f32_16x16x32_bf16 v[104:107], v[220:223], v[44:47], v[104:107]\n"
      "s_waitcnt lgkmcnt(1)\n"
      "v_mfma_f32_16x16x32_bf16 v[104:107], v[224:227], v[48:51], v[104:107]\n"
      "s_waitcnt lgkmcnt(0)\n"
      "v_mfma_f32_16x16x32_bf16 v[104:107], v[228:231], v[52:55], v[104:107]\n"
      "s_waitcnt vmcnt(4)\n"
      "s_add_u32 m0, s65, 0\n"
      "s_nop 0\n"
      "buffer_load_dwordx4 v35, s[16:19], s84 offen lds\n"
      "s_add_u32 m0, s65, 1024\n"
      "s_nop 0\n"
      "buffer_load_dwordx4 v36, s[16:19], s85 offen lds\n"
      "s_add_u32 m0, s65, 2048\n"
      "s_nop 0\n"
      "buffer_load_dwordx4 v37, s[16:19], s86 offen lds\n"
      "s_add_u32 m0, s65, 3072\n"
      "s_nop 0\n"
      "buffer_load_dwordx4 v38, s[16:19], s87 offen lds\n"
      "ds_read_b128 v[72:75], v112 offset:0\n"
      "ds_read_b128 v[76:79], v113 offset:0\n"
      "ds_read_b128 v[80:83], v114 offset:0\n"
      "ds_read_b128 v[84:87], v115 offset:0\n"
      "s_waitcnt lgkmcnt(3)\n"
      "v_mfma_f32_16x16x32_bf16 v[108:111], v[72:75], v[40:43], 0\n"
      "s_waitcnt lgkmcnt(2)\n"
      "v_mfma_f32_16x16x32_bf16 v[108:111], v[76:79], v[44:47], v[108:111]\n"
      "s_waitcnt lgkmcnt(1)\n"
      "v_mfma_f32_16x16x32_bf16 v[108:111], v[80:83], v[48:51], v[108:111]\n"
      "s_waitcnt lgkmcnt(0)\n"
      "v_mfma_f32_16x16x32_bf16 v[108:111], v[84:87], v[52:55], v[108:111]\n"
      "ds_read_b128 v[40:43], v116 offset:12288\n"
      "ds_read_b128 v[44:47], v117 offset:12288\n"
      "ds_read_b128 v[48:51], v118 offset:12288\n"
      "ds_read_b128 v[52:55], v119 offset:12288\n"
      "s_waitcnt lgkmcnt(3)\n"
      "v_mfma_f32_16x16x32_bf16 v[104:107], v[232:235], v[40:43], v[104:107]\n"
      "s_waitcnt lgkmcnt(2)\n"
      "v_mfma_f32_16x16x32_bf16 v[104:107], v[236:239], v[44:47], v[104:107]\n"
      "s_waitcnt lgkmcnt(1)\n"
      "v_mfma_f32_16x16x32_bf16 v[104:107], v[240:243], v[48:51], v[104:107]\n"
      "s_waitcnt lgkmcnt(0)\n"
      "v_mfma_f32_16x16x32_bf16 v[104:107], v[244:247], v[52:55], v[104:107]\n"
      "s_waitcnt vmcnt(4)\n"
      "s_add_u32 m0, s65, 32512\n"
      "s_nop 0\n"
      "buffer_load_dwordx4 v35, s[16:19], s84 offen offset:256 lds\n"
      "s_add_u32 m0, s65, 33536\n"
      "s_nop 0\n"
      "buffer_load_dwordx4 v36, s[16:19], s85 offen offset:256 lds\n"
      "s_add_u32 m0, s65, 34560\n"
      "s_nop 0\n"
      "buffer_load_dwordx4 v37, s[16:19], s86 offen offset:256 lds\n"
      "s_add_u32 m0, s65, 35584\n"
      "s_nop 0\n"
      "buffer_load_dwordx4 v38, s[16:19], s87 offen offset:256 lds\n"
      "ds_read_b128 v[72:75], v112 offset:32768\n"
      "v_subrev_u32 v112, 0x10000, v112\n"
      "ds_read_b128 v[76:79], v113 offset:32768\n"
      "v_subrev_u32 v113, 0x10000, v113\n"
      "ds_read_b128 v[80:83], v114 offset:32768\n"
      "v_subrev_u32 v114, 0x10000, v114\n"
      "ds_read_b128 v[84:87], v115 offset:32768\n"
      "v_subrev_u32 v115, 0x10000, v115\n"
      "s_waitcnt lgkmcnt(3)\n"
      "v_mfma_f32_16x16x32_bf16 v[108:111], v[72:75], v[40:43], v[108:111]\n"
      "s_waitcnt lgkmcnt(2)\n"
      "v_mfma_f32_16x16x32_bf16 v[108:111], v[76:79], v[44:47], v[108:111]\n"
      "s_waitcnt lgkmcnt(1)\n"
      "v_mfma_f32_16x16x32_bf16 v[108:111], v[80:83], v[48:51], v[108:111]\n"
      "s_waitcnt lgkmcnt(0)\n"
      "v_mfma_f32_16x16x32_bf16 v[108:111], v[84:87], v[52:55], v[108:111]\n"
      "ds_read_b128 v[40:43], v116 offset:0\n"
      "ds_read_b128 v[44:47], v117 offset:0\n"
      "ds_read_b128 v[48:51], v118 offset:0\n"
      "ds_read_b128 v[52:55], v119 offset:0\n"
      "s_waitcnt lgkmcnt(3)\n"
      "v_mfma_f32_16x16x32_bf16 v[104:107], v[184:187], v[40:43], v[104:107]\n"
      "s_waitcnt lgkmcnt(2)\n"
      "v_mfma_f32_16x16x32_bf16 v[104:107], v[188:191], v[44:47], v[104:107]\n"
      "s_waitcnt lgkmcnt(1)\n"
      "v_mfma_f32_16x16x32_bf16 v[104:107], v[192:195], v[48:51], v[104:107]\n"
      "s_waitcnt lgkmcnt(0)\n"
      "v_mfma_f32_16x16x32_bf16 v[104:107], v[196:199], v[52:55], v[104:107]\n"
      "s_waitcnt vmcnt(4)\n"
      "ds_read_b128 v[72:75], v112 offset:0\n"
      "ds_read_b128 v[76:79], v113 offset:0\n"
      "ds_read_b128 v[80:83], v114 offset:0\n"
      "ds_read_b128 v[84:87], v115 offset:0\n"
      "s_waitcnt lgkmcnt(3)\n"
      "v_mfma_f32_16x16x32_bf16 v[108:111], v[72:75], v[40:43], v[108:111]\n"
      "s_waitcnt lgkmcnt(2)\n"
      "v_mfma_f32_16x16x32_bf16 v[108:111], v[76:79], v[44:47], v[108:111]\n"
      "s_waitcnt lgkmcnt(1)\n"
      "v_mfma_f32_16x16x32_bf16 v[108:111], v[80:83], v[48:51], v[108:111]\n"
      "s_waitcnt lgkmcnt(0)\n"
      "v_mfma_f32_16x16x32_bf16 v[108:111], v[84:87], v[52:55], v[108:111]\n"
      "ds_read_b128 v[40:43], v116 offset:4096\n"
      "ds_read_b128 v[44:47], v117 offset:4096\n"
      "ds_read_b128 v[48:51], v118 offset:4096\n"
      "ds_read_b128 v[52:55], v119 offset:4096\n"
      "s_waitcnt lgkmcnt(3)\n"
      "v_mfma_f32_16x16x32_bf16 v[104:107], v[200:203], v[40:43], v[104:107]\n"
      "s_waitcnt lgkmcnt(2)\n"
      "v_mfma_f32_16x16x32_bf16 v[104:107], v[204:207], v[44:47], v[104:107]\n"
      "s_waitcnt lgkmcnt(1)\n"
      "v_mfma_f32_16x16x32_bf16 v[104:107], v[208:211], v[48:51], v[104:107]\n"
      "s_waitcnt lgkmcnt(0)\n"
      "v_mfma_f32_16x16x32_bf16 v[104:107], v[212:215], v[52:55], v[104:107]\n"
      "s_waitcnt vmcnt(0)\n"
      "ds_read_b128 v[72:75], v112 offset:32768\n"
      "ds_read_b128 v[76:79], v113 offset:32768\n"
      "ds_read_b128 v[80:83], v114 offset:32768\n"
      "ds_read_b128 v[84:87], v115 offset:32768\n"
      "s_waitcnt lgkmcnt(3)\n"
      "v_mfma_f32_16x16x32_bf16 v[108:111], v[72:75], v[40:43], v[108:111]\n"
      "s_waitcnt lgkmcnt(2)\n"
      "v_mfma_f32_16x16x32_bf16 v[108:111], v[76:79], v[44:47], v[108:111]\n"
      "s_waitcnt lgkmcnt(1)\n"
      "v_mfma_f32_16x16x32_bf16 v[108:111], v[80:83], v[48:51], v[108:111]\n"
      "s_waitcnt lgkmcnt(0)\n"
      "v_mfma_f32_16x16x32_bf16 v[108:111], v[84:87], v[52:55], v[108:111]\n"
      "v_max3_f32 v8, v104, v105, v106\n"
      "v_max_f32 v8, v8, v107\n"
      "s_nop 5\n"
      "v_max3_f32 v8, v8, v108, v109\n"
      "v_max3_f32 v8, v8, v110, v111\n"
      "v_mov_b32 v6, v8\n"
      "s_nop 1\n"
      "v_permlane16_swap_b32 v8, v6\n"
      "v_max_f32 v8, v8, v6\n"
      "v_mov_b32 v6, v8\n"
      "s_nop 1\n"
      "v_permlane32_swap_b32 v8, v6\n"
      "v_max_f32 v8, v8, v6\n"
      "ds_write_b32 v254, v8\n"
      "v_sub_f32 v104, v104, v8\n"
      "v_sub_f32 v105, v105, v8\n"
      "v_sub_f32 v106, v106, v8\n"
      "v_sub_f32 v107, v107, v8\n"
      "v_sub_f32 v108, v108, v8\n"
      "v_sub_f32 v109, v109, v8\n"
      "v_sub_f32 v110, v110, v8\n"
      "v_sub_f32 v111, v111, v8\n"
      "v_exp_f32 v104, v104\n"
      "v_exp_f32 v105, v105\n"
      "v_exp_f32 v106, v106\n"
      "v_exp_f32 v107, v107\n"
      "v_exp_f32 v108, v108\n"
      "v_exp_f32 v109, v109\n"
      "v_exp_f32 v110, v110\n"
      "v_exp_f32 v111, v111\n"
      "s_setprio 0\n"
      "s_waitcnt lgkmcnt(0)\n"
      "s_barrier\n"
      "buffer_load_dwordx4 v[56:59], v26, s[20:23], s54 offen nt\n"
      "s_add_u32 s54, s54, 131072\n"
      "buffer_load_dwordx4 v[60:63], v26, s[20:23], s54 offen nt\n"
      "s_add_u32 s54, s54, 131072\n"
      "buffer_load_dwordx4 v[64:67], v26, s[20:23], s54 offen nt\n"
      "s_add_u32 s54, s54, 131072\n"
      "buffer_load_dwordx4 v[68:71], v26, s[20:23], s54 offen nt\n"
      "ds_read_b128 v[40:43], v255\n"
      "ds_read_b128 v[44:47], v255 offset:16\n"
      "ds_read_b64_tr_b16 v[184:185], v248 offset:0\n"
      "ds_read_b64_tr_b16 v[186:187], v248 offset:4096\n"
      "ds_read_b64_tr_b16 v[200:201], v249 offset:0\n"
      "ds_read_b64_tr_b16 v[202:203], v249 offset:4096\n"
      "ds_read_b64_tr_b16 v[216:217], v250 offset:0\n"
      "ds_read_b64_tr_b16 v[218:219], v250 offset:4096\n"
      "ds_read_b64_tr_b16 v[232:233], v251 offset:0\n"
      "ds_read_b64_tr_b16 v[234:235], v251 offset:4096\n"
      "ds_read_b64_tr_b16 v[188:189], v248 offset:8192\n"
      "ds_read_b64_tr_b16 v[190:191], v248 offset:12288\n"
      "ds_read_b64_tr_b16 v[204:205], v249 offset:8192\n"
      "ds_read_b64_tr_b16 v[206:207], v249 offset:12288\n"
      "v_pk_add_f32 v[48:49], v[104:105], v[106:107]\n"
      "v_pk_add_f32 v[50:51], v[108:109], v[110:111]\n"
      "v_pk_add_f32 v[48:49], v[48:49], v[50:51]\n"
      "v_add_f32 v9, v48, v49\n"
      "v_mov_b32 v6, v9\n"
      "s_nop 1\n"
      "v_permlane16_swap_b32 v9, v6\n"
      "v_add_f32 v9, v9, v6\n"
      "v_mov_b32 v6, v9\n"
      "s_nop 1\n"
      "v_permlane32_swap_b32 v9, v6\n"
      "v_add_f32 v9, v9, v6\n"
      "s_waitcnt lgkmcnt(12)\n"
      "v_max3_f32 v40, v40, v41, v42\n"
      "v_max3_f32 v44, v44, v45, v46\n"
      "v_max3_f32 v40, v40, v43, v47\n"
      "v_max_f32 v112, v40, v44\n"
      "v_sub_f32 v8, v8, v112\n"
      "v_exp_f32 v8, v8\n"
      "s_nop 0\n"
      "v_pk_mul_f32 v[104:105], v[104:105], v[8:9] op_sel_hi:[1,0]\n"
      "v_pk_mul_f32 v[106:107], v[106:107], v[8:9] op_sel_hi:[1,0]\n"
      "v_pk_mul_f32 v[108:109], v[108:109], v[8:9] op_sel_hi:[1,0]\n"
      "v_pk_mul_f32 v[110:111], v[110:111], v[8:9] op_sel_hi:[1,0]\n"
      "v_mul_f32 v9, v9, v8\n"
      "v_cvt_pk_bf16_f32 v104, v104, v105\n"
      "v_cvt_pk_bf16_f32 v105, v106, v107\n"
      "v_cvt_pk_bf16_f32 v106, v108, v109\n"
      "v_cvt_pk_bf16_f32 v107, v110, v111\n"
      "ds_write_b64 v252, v[104:105]\n"
      "ds_write_b64 v252, v[106:107] offset:4096\n"
      "ds_write_b32 v254, v9 offset:512\n"
      "s_waitcnt lgkmcnt(0)\n"
      "s_barrier\n"
      "ds_read_b128 v[72:75], v253 offset:0\n"
      "ds_read_b128 v[76:79], v253 offset:1024\n"
      "ds_read_b128 v[80:83], v253 offset:2048\n"
      "ds_read_b128 v[84:87], v253 offset:3072\n"
      "s_waitcnt lgkmcnt(3)\n"
      "v_mfma_f32_16x16x32_bf16 v[40:43], v[120:123], v[72:75], 0\n"
      "ds_read_b128 v[88:91], v253 offset:4096\n"
      "ds_read_b128 v[92:95], v253 offset:5120\n"
      "v_mfma_f32_16x16x32_bf16 v[44:47], v[136:139], v[72:75], 0\n"
      "ds_read_b64_tr_b16 v[220:221], v250 offset:8192\n"
      "ds_read_b64_tr_b16 v[222:223], v250 offset:12288\n"
      "v_mfma_f32_16x16x32_bf16 v[48:51], v[152:155], v[72:75], 0\n"
      "ds_read_b64_tr_b16 v[236:237], v251 offset:8192\n"
      "ds_read_b64_tr_b16 v[238:239], v251 offset:12288\n"
      "v_mfma_f32_16x16x32_bf16 v[52:55], v[168:171], v[72:75], 0\n"
      "ds_read_b128 v[96:99], v253 offset:6144\n"
      "ds_read_b64_tr_b16 v[192:193], v248 offset:16384\n"
      "s_waitcnt lgkmcnt(10)\n"
      "v_mfma_f32_16x16x32_bf16 v[40:43], v[124:127], v[76:79], v[40:43]\n"
      "ds_read_b64_tr_b16 v[194:195], v248 offset:20480\n"
      "ds_read_b64_tr_b16 v[208:209], v249 offset:16384\n"
      "v_mfma_f32_16x16x32_bf16 v[44:47], v[140:143], v[76:79], v[44:47]\n"
      "ds_read_b64_tr_b16 v[210:211], v249 offset:20480\n"
      "ds_read_b64_tr_b16 v[224:225], v250 offset:16384\n"
      "v_mfma_f32_16x16x32_bf16 v[48:51], v[156:159], v[76:79], v[48:51]\n"
      "ds_read_b64_tr_b16 v[226:227], v250 offset:20480\n"
      "ds_read_b64_tr_b16 v[240:241], v251 offset:16384\n"
      "v_mfma_f32_16x16x32_bf16 v[52:55], v[172:175], v[76:79], v[52:55]\n"
      "ds_read_b64_tr_b16 v[242:243], v251 offset:20480\n"
      "ds_read_b128 v[100:103], v253 offset:7168\n"
      "s_waitcnt lgkmcnt(14)\n"
      "v_mfma_f32_16x16x32_bf16 v[40:43], v[128:131], v[80:83], v[40:43]\n"
      "ds_read_b64_tr_b16 v[196:197], v248 offset:24576\n"
      "ds_read_b64_tr_b16 v[198:199], v248 offset:28672\n"
      "v_mfma_f32_16x16x32_bf16 v[44:47], v[144:147], v[80:83], v[44:47]\n"
      "ds_read_b64_tr_b16 v[212:213], v249 offset:24576\n"
      "ds_read_b64_tr_b16 v[214:215], v249 offset:28672\n"
      "v_mfma_f32_16x16x32_bf16 v[48:51], v[160:163], v[80:83], v[48:51]\n"
      "ds_read_b64_tr_b16 v[228:229], v250 offset:24576\n"
      "ds_read_b64_tr_b16 v[230:231], v250 offset:28672\n"
      "v_mfma_f32_16x16x32_bf16 v[52:55], v[176:179], v[80:83], v[52:55]\n"
      "ds_read_b64_tr_b16 v[244:245], v251 offset:24576\n"
      "ds_read_b64_tr_b16 v[246:247], v251 offset:28672\n"
      "v_mfma_f32_16x16x32_bf16 v[40:43], v[132:135], v[84:87], v[40:43]\n"
      "v_mfma_f32_16x16x32_bf16 v[44:47], v[148:151], v[84:87], v[44:47]\n"
      "v_mfma_f32_16x16x32_bf16 v[48:51], v[164:167], v[84:87], v[48:51]\n"
      "v_mfma_f32_16x16x32_bf16 v[52:55], v[180:183], v[84:87], v[52:55]\n"
      "v_mfma_f32_16x16x32_bf16 v[40:43], v[184:187], v[88:91], v[40:43]\n"
      "v_mfma_f32_16x16x32_bf16 v[44:47], v[200:203], v[88:91], v[44:47]\n"
      "v_mfma_f32_16x16x32_bf16 v[48:51], v[216:219], v[88:91], v[48:51]\n"
      "v_mfma_f32_16x16x32_bf16 v[52:55], v[232:235], v[88:91], v[52:55]\n"
      "v_mfma_f32_16x16x32_bf16 v[40:43], v[188:191], v[92:95], v[40:43]\n"
      "v_mfma_f32_16x16x32_bf16 v[44:47], v[204:207], v[92:95], v[44:47]\n"
      "s_waitcnt lgkmcnt(14)\n"
      "v_mfma_f32_16x16x32_bf16 v[48:51], v[220:223], v[92:95], v[48:51]\n"
      "v_mfma_f32_16x16x32_bf16 v[52:55], v[236:239], v[92:95], v[52:55]\n"
      "v_mfma_f32_16x16x32_bf16 v[40:43], v[192:195], v[96:99], v[40:43]\n"
      "s_waitcnt lgkmcnt(13)\n"
      "v_mfma_f32_16x16x32_bf16 v[44:47], v[208:211], v[96:99], v[44:47]\n"
      "s_waitcnt lgkmcnt(11)\n"
      "v_mfma_f32_16x16x32_bf16 v[48:51], v[224:227], v[96:99], v[48:51]\n"
      "s_waitcnt lgkmcnt(9)\n"
      "v_mfma_f32_16x16x32_bf16 v[52:55], v[240:243], v[96:99], v[52:55]\n"
      "s_waitcnt lgkmcnt(6)\n"
      "v_mfma_f32_16x16x32_bf16 v[40:43], v[196:199], v[100:103], v[40:43]\n"
      "s_waitcnt lgkmcnt(4)\n"
      "v_mfma_f32_16x16x32_bf16 v[44:47], v[212:215], v[100:103], v[44:47]\n"
      "s_waitcnt lgkmcnt(2)\n"
      "v_mfma_f32_16x16x32_bf16 v[48:51], v[228:231], v[100:103], v[48:51]\n"
      "s_waitcnt lgkmcnt(0)\n"
      "v_mfma_f32_16x16x32_bf16 v[52:55], v[244:247], v[100:103], v[52:55]\n"
      "s_mov_b32 s52, 532480\n"
      "v_xor_b32 v5, 16, v27\n"
      "v_cvt_pk_bf16_f32 v104, v40, v41\n"
      "v_cvt_pk_bf16_f32 v105, v42, v43\n"
      "v_cvt_pk_bf16_f32 v106, v44, v45\n"
      "v_cvt_pk_bf16_f32 v107, v46, v47\n"
      "ds_write_b128 v27, v[104:107]\n"
      "v_cvt_pk_bf16_f32 v108, v48, v49\n"
      "v_cvt_pk_bf16_f32 v109, v50, v51\n"
      "v_cvt_pk_bf16_f32 v110, v52, v53\n"
      "v_cvt_pk_bf16_f32 v111, v54, v55\n"
      "ds_write_b128 v5, v[108:111]\n"
      "ds_read_b128 v[72:75], v28 offset:0\n"
      "ds_read_b128 v[76:79], v28 offset:1024\n"
      "s_waitcnt lgkmcnt(1)\n"
      "buffer_store_dwordx4 v[72:75], v29, s[12:15], 0 offen sc1\n"
      "s_waitcnt lgkmcnt(0)\n"
      "buffer_store_dwordx4 v[76:79], v29, s[12:15], s52 offen sc1\n"
      "s_cmp_lg_u32 s36, 1\n"
      "s_cbranch_scc1 .Lfd_Bnm\n"
      "ds_read_b128 v[104:107], v255 offset:512\n"
      "ds_read_b128 v[108:111], v255 offset:528\n"
      "s_waitcnt lgkmcnt(0)\n"
      "v_pk_add_f32 v[104:105], v[104:105], v[106:107]\n"
      "v_pk_add_f32 v[108:109], v[108:109], v[110:111]\n"
      "v_pk_add_f32 v[104:105], v[104:105], v[108:109]\n"
      "v_add_f32 v113, v104, v105\n"
      "s_mov_b32 exec_lo, 0xffff\n"
      "s_mov_b32 exec_hi, 0\n"
      "buffer_store_dwordx2 v[112:113], v30, s[12:15], 0 offen sc1\n"
      "s_mov_b64 exec, -1\n"
      ".Lfd_Bnm:\n"
      "s_lshl_b32 s57, s3, 15\n"
      "s_add_u32 s57, 0xa1000, s57\n"
      "s_bfe_u32 s58, s41, 0x30003\n"
      "s_lshl_b32 s58, s58, 12\n"
      "s_add_u32 s58, s57, s58\n"
      "s_lshr_b32 s57, s41, 6\n"
      "s_lshl_b32 s57, s57, 6\n"
      "s_add_u32 s57, s58, s57\n"
      "s_bfe_u32 s58, s41, 0x20001\n"
      "s_lshl_b32 s58, s58, 2\n"
      "s_add_u32 s58, s57, s58\n"
      "v_lshl_add_u32 v150, v0, 7, s58\n"
      "s_mul_i32 s58, s36, 0x1200\n"
      "s_add_u32 s58, 0, s58\n"
      "v_lshl_add_u32 v151, v0, 4, s58\n"
      "s_lshr_b32 s58, s38, 6\n"
      "s_lshl_b32 s58, s58, 15\n"
      "s_add_u32 s58, 0xa1000, s58\n"
      "s_lshl_b32 s57, s36, 12\n"
      "s_add_u32 s57, s58, s57\n"
      "s_and_b32 s58, s38, 7\n"
      "s_lshl_b32 s58, s58, 7\n"
      "s_add_u32 s64, s57, s58\n"
      "s_lshl_b32 s58, s39, 9\n"
      "s_add_u32 s58, 0xf6000, s58\n"
      "s_lshl_b32 s57, s36, 6\n"
      "s_add_u32 s57, s58, s57\n"
      "v_lshl_add_u32 v141, v1, 4, s57\n"
      "s_mul_i32 s57, s38, 0x4100\n"
      "s_add_u32 s57, 0x2a4400, s57\n"
      "s_lshl_b32 s58, s36, 10\n"
      "s_add_u32 s58, s57, s58\n"
      "v_lshl_add_u32 v142, v0, 4, s58\n"
      "v_lshrrev_b32 v140, 5, v0\n"
      "v_mul_u32_u24 v5, 0x900, v1\n"
      "v_lshl_add_u32 v5, v3, 8, v5\n"
      "s_lshl_b32 s58, s36, 1\n"
      "v_lshrrev_b32 v7, 1, v4\n"
      "v_add_u32 v7, s58, v7\n"
      "v_lshlrev_b32 v6, 1, v3\n"
      "v_xor_b32 v6, v7, v6\n"
      "v_lshl_add_u32 v5, v6, 4, v5\n"
      "v_and_b32 v6, 1, v4\n"
      "v_lshl_add_u32 v143, v6, 3, v5\n"
      "s_mul_i32 s58, s36, 0x220\n"
      "s_add_u32 s58, 0x20000, s58\n"
      "v_mul_u32_u24 v5, 0x110, v140\n"
      "v_add_u32 v5, s58, v5\n"
      "v_lshl_add_u32 v5, v2, 4, v5\n"
      "v_and_b32 v6, 1, v1\n"
      "v_lshl_add_u32 v144, v6, 3, v5\n"
      "v_mul_u32_u24 v5, 0x110, v2\n"
      "v_add_u32 v5, 0x20000, v5\n"
      "v_lshl_add_u32 v145, v1, 4, v5\n"
      "s_lshl_b32 s58, s38, 10\n"
      "s_add_u32 s58, 0xfe000, s58\n"
      "s_lshl_b32 s57, s36, 6\n"
      "s_add_u32 s57, s58, s57\n"
      "v_lshl_add_u32 v5, v140, 9, s57\n"
      "v_lshl_add_u32 v146, v2, 2, v5\n"
      "s_lshl_b32 s57, s36, 4\n"
      "s_add_u32 s57, 0x24800, s57\n"
      "v_mov_b32 v147, s57\n"
      "v_lshlrev_b32 v5, 5, v1\n"
      "v_add_u32 v148, 0x24800, v5\n"
      "s_lshl_b32 s57, s39, 7\n"
      "s_add_u32 s57, 0xf1000, s57\n"
      "s_add_u32 s57, s57, 64\n"
      "s_lshl_b32 s58, s40, 2\n"
      "s_add_u32 s58, s57, s58\n"
      "v_mov_b32 v149, s58\n"
      "s_mov_b32 s52, 8192\n"
      "s_waitcnt vmcnt(0)\n"
      "s_waitcnt lgkmcnt(0)\n"
      "s_barrier\n"
      "s_cmp_lg_u32 s36, 4\n"
      "s_cbranch_scc1 .Lfd_Bna\n"
      "s_mov_b32 exec_lo, 0xff\n"
      "s_mov_b32 exec_hi, 0\n"
      "buffer_store_dword v10, v150, s[12:15], 0 offen sc1\n"
      "s_mov_b64 exec, -1\n"
      ".Lfd_Bna:\n"
      "ds_write_b128 v151, v[56:59] offset:0\n"
      "ds_write_b128 v151, v[60:63] offset:1152\n"
      "ds_write_b128 v151, v[64:67] offset:2304\n"
      "ds_write_b128 v151, v[68:71] offset:3456\n"
      "s_waitcnt lgkmcnt(0)\n"
      "s_barrier\n"
      "ds_read_b64_tr_b16 v[120:121], v143 offset:0\n"
      "ds_read_b64_tr_b16 v[122:123], v143 offset:1152\n"
      "ds_read_b64_tr_b16 v[124:125], v143 offset:9216\n"
      "ds_read_b64_tr_b16 v[126:127], v143 offset:10368\n"
      "ds_read_b64_tr_b16 v[128:129], v143 offset:18432\n"
      "ds_read_b64_tr_b16 v[130:131], v143 offset:19584\n"
      "ds_read_b64_tr_b16 v[132:133], v143 offset:27648\n"
      "ds_read_b64_tr_b16 v[134:135], v143 offset:28800\n"
      "s_mov_b32 s61, 0\n"
      ".Lfd_p_18:\n"
      "s_buffer_load_dwordx4 s[68:71], s[12:15], s64 glc\n"
      "s_buffer_load_dwordx4 s[72:75], s[12:15], s64 offset:64 glc\n"
      "s_waitcnt lgkmcnt(0)\n"
      "s_cmp_eq_u64 s[68:69], s[42:43]\n"
      "s_cbranch_scc0 .Lfd_r_19\n"
      "s_cmp_eq_u64 s[70:71], s[42:43]\n"
      "s_cbranch_scc0 .Lfd_r_19\n"
      "s_cmp_eq_u64 s[72:73], s[42:43]\n"
      "s_cbranch_scc0 .Lfd_r_19\n"
      "s_cmp_eq_u64 s[74:75], s[42:43]\n"
      "s_cbranch_scc0 .Lfd_r_19\n"
      ".Lfd_d_20:\n"
      "buffer_load_dwordx4 v[48:51], v141, s[12:15], 0 offen sc1\n"
      "buffer_load_dwordx4 v[72:75], v142, s[12:15], 0 offen sc1\n"
      "buffer_load_dwordx4 v[76:79], v142, s[12:15], s52 offen sc1\n"
      "s_waitcnt vmcnt(2)\n"
      "v_mov_b32 v64, v48\n"
      "v_mov_b32 v66, v50\n"
      "v_mov_b32 v5, v64\n"
      "v_mov_b32 v6, v66\n"
      "s_nop 0\n"
      "v_permlane16_swap_b32 v64, v5\n"
      "v_permlane16_swap_b32 v66, v6\n"
      "v_max_f32 v64, v64, v5\n"
      "v_max_f32 v66, v66, v6\n"
      "v_mov_b32 v5, v64\n"
      "v_mov_b32 v6, v66\n"
      "s_nop 0\n"
      "v_permlane32_swap_b32 v64, v5\n"
      "v_permlane32_swap_b32 v66, v6\n"
      "v_max_f32 v64, v64, v5\n"
      "v_max_f32 v66, v66, v6\n"
      "v_sub_f32 v48, v48, v64\n"
      "v_sub_f32 v50, v50, v66\n"
      "v_exp_f32 v48, v48\n"
      "v_exp_f32 v50, v50\n"
      "v_mul_f32 v65, v48, v49\n"
      "v_mul_f32 v67, v50, v51\n"
      "v_mov_b32 v5, v65\n"
      "v_mov_b32 v6, v67\n"
      "s_nop 0\n"
      "v_permlane16_swap_b32 v65, v5\n"
      "v_permlane16_swap_b32 v67, v6\n"
      "v_add_f32 v65, v65, v5\n"
      "v_add_f32 v67, v67, v6\n"
      "v_mov_b32 v5, v65\n"
      "v_mov_b32 v6, v67\n"
      "s_nop 0\n"
      "v_permlane32_swap_b32 v65, v5\n"
      "v_permlane32_swap_b32 v67, v6\n"
      "v_add_f32 v65, v65, v5\n"
      "v_add_f32 v67, v67, v6\n"
      "ds_write_b128 v147, v[64:67]\n"
      "s_waitcnt vmcnt(1)\n"
      "v_lshlrev_b32 v88, 16, v72\n"
      "v_and_b32 v89, 0xffff0000, v72\n"
      "v_lshlrev_b32 v90, 16, v73\n"
      "v_and_b32 v91, 0xffff0000, v73\n"
      "v_lshlrev_b32 v92, 16, v74\n"
      "v_and_b32 v93, 0xffff0000, v74\n"
      "v_lshlrev_b32 v94, 16, v75\n"
      "v_and_b32 v95, 0xffff0000, v75\n"
      "v_pk_mul_f32 v[88:89], v[88:89], v[48:49] op_sel_hi:[1,0]\n"
      "v_pk_mul_f32 v[90:91], v[90:91], v[48:49] op_sel_hi:[1,0]\n"
      "v_pk_mul_f32 v[92:93], v[92:93], v[48:49] op_sel_hi:[1,0]\n"
      "v_pk_mul_f32 v[94:95], v[94:95], v[48:49] op_sel_hi:[1,0]\n"
      "s_waitcnt vmcnt(0)\n"
      "s_add_u32 m0, s46, 0\n"
      "s_add_u32 s55, s63, 0\n"
      "buffer_load_dwordx4 v24, s[28:31], s55 offen nt lds\n"
      "s_add_u32 m0, s46, 1024\n"
      "s_add_u32 s55, s63, 9216\n"
      "buffer_load_dwordx4 v24, s[28:31], s55 offen nt lds\n"
      "s_add_u32 m0, s46, 2048\n"
      "s_add_u32 s55, s63, 18432\n"
      "buffer_load_dwordx4 v24, s[28:31], s55 offen nt lds\n"
      "s_add_u32 m0, s46, 3072\n"
      "s_add_u32 s55, s63, 27648\n"
      "buffer_load_dwordx4 v24, s[28:31], s55 offen nt lds\n"
      "v_lshlrev_b32 v96, 16, v76\n"
      "v_and_b32 v97, 0xffff0000, v76\n"
      "v_lshlrev_b32 v98, 16, v77\n"
      "v_and_b32 v99, 0xffff0000, v77\n"
      "v_lshlrev_b32 v100, 16, v78\n"
      "v_and_b32 v101, 0xffff0000, v78\n"
      "v_lshlrev_b32 v102, 16, v79\n"
      "v_and_b32 v103, 0xffff0000, v79\n"
      "v_pk_mul_f32 v[96:97], v[96:97], v[50:51] op_sel_hi:[1,0]\n"
      "v_pk_mul_f32 v[98:99], v[98:99], v[50:51] op_sel_hi:[1,0]\n"
      "v_pk_mul_f32 v[100:101], v[100:101], v[50:51] op_sel_hi:[1,0]\n"
      "v_pk_mul_f32 v[102:103], v[102:103], v[50:51] op_sel_hi:[1,0]\n"
      "v_permlane32_swap_b32 v88, v96\n"
      "v_permlane32_swap_b32 v89, v97\n"
      "v_permlane32_swap_b32 v90, v98\n"
      "v_permlane32_swap_b32 v91, v99\n"
      "v_permlane32_swap_b32 v92, v100\n"
      "v_permlane32_swap_b32 v93, v101\n"
      "v_permlane32_swap_b32 v94, v102\n"
      "v_permlane32_swap_b32 v95, v103\n"
      "v_pk_add_f32 v[88:89], v[88:89], v[96:97]\n"
      "v_pk_add_f32 v[90:91], v[90:91], v[98:99]\n"
      "v_pk_add_f32 v[92:93], v[92:93], v[100:101]\n"
      "v_pk_add_f32 v[94:95], v[94:95], v[102:103]\n"
      "s_nop 0\n"
      "v_permlane16_swap_b32 v88, v92\n"
      "v_permlane16_swap_b32 v89, v93\n"
      "v_permlane16_swap_b32 v90, v94\n"
      "v_permlane16_swap_b32 v91, v95\n"
      "v_pk_add_f32 v[88:89], v[88:89], v[92:93]\n"
      "v_pk_add_f32 v[90:91], v[90:91], v[94:95]\n"
      "v_cvt_pk_bf16_f32 v88, v88, v89\n"
      "v_cvt_pk_bf16_f32 v89, v90, v91\n"
      "ds_write_b64 v144, v[88:89]\n"
      "s_waitcnt lgkmcnt(0)\n"
      "s_barrier\n"
      "s_cmp_lg_u32 s36, 5\n"
      "s_cbranch_scc1 .Lfd_Bnx\n"
      "s_mov_b32 exec_lo, 1\n"
      "s_mov_b32 exec_hi, 0\n"
      "buffer_store_dword v10, v149, s[12:15], 0 offen sc1\n"
      "s_mov_b64 exec, -1\n"
      ".Lfd_Bnx:\n"
      "ds_read_b128 v[48:51], v148\n"
      "ds_read_b128 v[52:55], v148 offset:16\n"
      "ds_read_b128 v[72:75], v145 offset:0\n"
      "ds_read_b128 v[76:79], v145 offset:64\n"
      "ds_read_b128 v[80:83], v145 offset:128\n"
      "ds_read_b128 v[84:87], v145 offset:192\n"
      "s_cmp_lg_u32 s36, 7\n"
      "s_cbranch_scc1 .Lfd_w9_22\n"
      "s_add_u32 m0, s46, 4096\n"
      "s_add_u32 s55, s63, 128\n"
      "buffer_load_dwordx4 v24, s[28:31], s55 offen nt lds\n"
      "s_add_u32 m0, s46, 5120\n"
      "s_add_u32 s55, s63, 9344\n"
      "buffer_load_dwordx4 v24, s[28:31], s55 offen nt lds\n"
      "s_add_u32 m0, s46, 6144\n"
      "s_add_u32 s55, s63, 18560\n"
      "buffer_load_dwordx4 v24, s[28:31], s55 offen nt lds\n"
      "s_add_u32 m0, s46, 7168\n"
      "s_add_u32 s55, s63, 27776\n"
      "buffer_load_dwordx4 v24, s[28:31], s55 offen nt lds\n"
      ".Lfd_w9_22:\n"
      "s_waitcnt lgkmcnt(4)\n"
      "v_max_f32 v56, v48, v52\n"
      "v_max_f32 v57, v50, v54\n"
      "s_waitcnt lgkmcnt(3)\n"
      "v_mfma_f32_16x16x32_bf16 v[88:91], v[72:75], v[120:123], 0\n"
      "s_waitcnt lgkmcnt(2)\n"
      "v_mfma_f32_16x16x32_bf16 v[92:95], v[76:79], v[124:127], 0\n"
      "s_waitcnt lgkmcnt(1)\n"
      "v_mfma_f32_16x16x32_bf16 v[88:91], v[80:83], v[128:131], v[88:91]\n"
      "s_waitcnt lgkmcnt(0)\n"
      "v_mfma_f32_16x16x32_bf16 v[92:95], v[84:87], v[132:135], v[92:95]\n"
      "v_mov_b32 v5, v56\n"
      "v_mov_b32 v6, v57\n"
      "s_nop 0\n"
      "v_permlane16_swap_b32 v56, v5\n"
      "v_permlane16_swap_b32 v57, v6\n"
      "v_max_f32 v56, v56, v5\n"
      "v_max_f32 v57, v57, v6\n"
      "v_mov_b32 v5, v56\n"
      "v_mov_b32 v6, v57\n"
      "s_nop 0\n"
      "v_permlane32_swap_b32 v56, v5\n"
      "v_permlane32_swap_b32 v57, v6\n"
      "v_max_f32 v56, v56, v5\n"
      "v_max_f32 v57, v57, v6\n"
      "v_sub_f32 v48, v48, v56\n"
      "v_sub_f32 v50, v50, v57\n"
      "v_sub_f32 v52, v52, v56\n"
      "v_sub_f32 v54, v54, v57\n"
      "v_exp_f32 v48, v48\n"
      "v_exp_f32 v50, v50\n"
      "v_exp_f32 v52, v52\n"
      "v_exp_f32 v54, v54\n"
      "v_pk_add_f32 v[88:89], v[88:89], v[92:93]\n"
      "v_pk_add_f32 v[90:91], v[90:91], v[94:95]\n"
      "v_mul_f32 v58, v48, v88\n"
      "v_mul_f32 v60, v48, v49\n"
      "v_mul_f32 v59, v50, v89\n"
      "v_mul_f32 v61, v50, v51\n"
      "v_fmac_f32 v58, v52, v90\n"
      "v_fmac_f32 v60, v52, v53\n"
      "v_fmac_f32 v59, v54, v91\n"
      "v_fmac_f32 v61, v54, v55\n"
      "s_nop 0\n"
      "v_permlane32_swap_b32 v58, v59\n"
      "v_permlane32_swap_b32 v60, v61\n"
      "v_add_f32 v58, v58, v59\n"
      "v_add_f32 v60, v60, v61\n"
      "v_mov_b32 v5, v60\n"
      "v_mov_b32 v6, v58\n"
      "s_nop 0\n"
      "v_permlane16_swap_b32 v60, v5\n"
      "v_permlane16_swap_b32 v58, v6\n"
      "v_add_f32 v60, v60, v5\n"
      "v_add_f32 v58, v58, v6\n"
      "v_rcp_f32 v60, v60\n"
      "s_nop 0\n"
      "v_mul_f32 v58, v58, v60\n"
      "s_nop 1\n"
      "v_mov_b32_dpp v5, v58 quad_perm:[1,0,3,2] row_mask:0xf bank_mask:0xf\n"
      "v_cvt_pk_bf16_f32 v32, v58, v5\n"
      "s_mov_b32 exec_lo, 0x5555\n"
      "s_mov_b32 exec_hi, 0x5555\n"
      "buffer_store_dwordx2 v[32:33], v146, s[12:15], 0 offen sc1\n"
      "s_mov_b64 exec, -1\n"
      "s_cmp_lg_u32 s36, 4\n"
      "s_cbranch_scc1 .Lfd_tailw\n"
      "s_lshl_b32 s58, s39, 7\n"
      "s_add_u32 s58, 0xf1000, s58\n"
      "s_add_u32 s52, s58, 64\n"
      "s_mov_b32 s61, 0\n"
      ".Lfd_p_23:\n"
      "s_buffer_load_dwordx4 s[68:71], s[12:15], s52 glc\n"
      "s_waitcnt lgkmcnt(0)\n"
      "s_cmp_eq_u64 s[68:69], s[42:43]\n"
      "s_cbranch_scc0 .Lfd_r_24\n"
      "s_cmp_eq_u64 s[70:71], s[42:43]\n"
      "s_cbranch_scc0 .Lfd_r_24\n"
      ".Lfd_d_25:\n"
      "s_branch .Lfd_tailc0\n"
      ".Lfd_r_19:\n"
      "s_sleep 4\n"
      "s_add_u32 s61, s61, 1\n"
      "s_cmp_lt_u32 s61, 4096\n"
      "s_cbranch_scc1 .Lfd_p_18\n"
      "v_and_b32 v5, 3, v0\n"
      "v_lshlrev_b32 v5, 2, v5\n"
      "v_bfe_u32 v6, v0, 2, 1\n"
      "v_lshl_add_u32 v5, v6, 6, v5\n"
      ".Lfd_v_21:\n"
      "buffer_load_dword v6, v5, s[12:15], s64 offen sc1\n"
      "s_waitcnt vmcnt(0)\n"
      "v_cmp_ne_u32 vcc, s43, v6\n"
      "s_cbranch_vccz .Lfd_d_20\n"
      "s_add_u32 s47, s47, 1\n"
      "s_cmp_lt_u32 s47, 0x10000\n"
      "s_cbranch_scc1 .Lfd_v_21\n"
      "s_branch .Lfd_d_20\n"
      ".Lfd_r_24:\n"
      "s_add_u32 s61, s61, 1\n"
      "s_cmp_lt_u32 s61, 4096\n"
      "s_cbranch_scc1 .Lfd_p_23\n"
      "s_branch .Lfd_d_25\n"
      ".Lfd_sub_b:\n"
      "s_nop 7\n"
      "v_lshlrev_b32 v5, 8, v2\n"
      "v_and_b32 v6, 7, v2\n"
      "v_lshrrev_b32 v7, 3, v2\n"
      "v_lshlrev_b32 v7, 2, v7\n"
      "v_xor_b32 v7, v6, v7\n"
      "v_lshlrev_b32 v7, 1, v7\n"
      "v_lshrrev_b32 v6, 3, v2\n"
      "v_or_b32 v6, v7, v6\n"
      "v_xor_b32 v6, v1, v6\n"
      "v_lshl_add_u32 v116, v6, 4, v5\n"
      "s_lshl_b32 s52, s36, 12\n"
      "v_add_u32 v112, s52, v116\n"
      "v_add_u32 v116, 131072, v116\n"
      "v_xor_b32 v113, 64, v112\n"
      "v_xor_b32 v117, 64, v116\n"
      "v_xor_b32 v114, 128, v112\n"
      "v_xor_b32 v118, 128, v116\n"
      "v_xor_b32 v115, 192, v112\n"
      "v_xor_b32 v119, 192, v116\n"
      "s_lshr_b32 s58, s36, 1\n"
      "s_lshl_b32 s58, s58, 15\n"
      "v_lshl_add_u32 v5, v1, 2, v3\n"
      "v_lshl_add_u32 v5, v5, 8, s58\n"
      "s_and_b32 s58, s36, 1\n"
      "s_lshl_b32 s58, s58, 3\n"
      "v_lshrrev_b32 v6, 1, v4\n"
      "v_add_u32 v6, s58, v6\n"
      "v_lshlrev_b32 v7, 1, v3\n"
      "v_xor_b32 v7, v6, v7\n"
      "v_lshrrev_b32 v6, 1, v1\n"
      "v_xor_b32 v6, v1, v6\n"
      "v_and_b32 v6, 1, v6\n"
      "v_lshlrev_b32 v6, 3, v6\n"
      "v_xor_b32 v6, v7, v6\n"
      "v_lshrrev_b32 v7, 1, v1\n"
      "v_xor_b32 v7, v6, v7\n"
      "v_lshl_add_u32 v5, v7, 4, v5\n"
      "v_and_b32 v7, 1, v4\n"
      "v_lshl_add_u32 v248, v7, 3, v5\n"
      "v_xor_b32 v249, 32, v248\n"
      "v_xor_b32 v250, 64, v248\n"
      "v_xor_b32 v251, 96, v248\n"
      "s_lshr_b32 s58, s36, 1\n"
      "s_lshl_b32 s58, s58, 10\n"
      "s_add_u32 s58, 0x25200, s58\n"
      "s_and_b32 s57, s36, 1\n"
      "s_lshl_b32 s57, s57, 3\n"
      "s_add_u32 s57, s58, s57\n"
      "v_lshl_add_u32 v252, v0, 4, s57\n"
      "v_lshlrev_b32 v5, 4, v0\n"
      "v_add_u32 v253, 0x25200, v5\n"
      "s_lshl_b32 s57, s36, 2\n"
      "s_add_u32 s57, 0x24400, s57\n"
      "v_lshl_add_u32 v254, v2, 5, s57\n"
      "v_lshlrev_b32 v5, 5, v2\n"
      "v_add_u32 v255, 0x24400, v5\n"
      "s_lshl_b32 s57, s36, 11\n"
      "s_add_u32 s57, 0x20000, s57\n"
      "v_lshl_add_u32 v5, v2, 7, s57\n"
      "v_lshlrev_b32 v7, 1, v1\n"
      "v_and_b32 v6, 7, v2\n"
      "v_xor_b32 v6, v7, v6\n"
      "v_lshl_add_u32 v27, v6, 4, v5\n"
      "s_lshl_b32 s57, s36, 11\n"
      "s_add_u32 s57, 0x20000, s57\n"
      "v_lshrrev_b32 v5, 3, v0\n"
      "v_lshl_add_u32 v5, v5, 7, s57\n"
      "v_and_b32 v6, 7, v0\n"
      "v_lshrrev_b32 v7, 3, v0\n"
      "v_xor_b32 v7, v6, v7\n"
      "v_lshl_add_u32 v28, v7, 4, v5\n"
      "s_lshl_b32 s57, s3, 6\n"
      "s_lshr_b32 s58, s36, 1\n"
      "s_add_u32 s58, s57, s58\n"
      "s_mul_i32 s58, s58, 0x4100\n"
      "s_add_u32 s58, 0x2a4400, s58\n"
      "s_lshr_b32 s57, s41, 1\n"
      "s_lshl_b32 s57, s57, 8\n"
      "s_add_u32 s57, s58, s57\n"
      "s_and_b32 s58, s36, 1\n"
      "s_lshl_b32 s58, s58, 7\n"
      "s_add_u32 s58, s57, s58\n"
      "v_lshrrev_b32 v5, 3, v0\n"
      "v_mul_u32_u24 v5, 0x10400, v5\n"
      "v_add_u32 v5, s58, v5\n"
      "v_and_b32 v7, 7, v0\n"
      "v_lshl_add_u32 v29, v7, 4, v5\n"
      "s_lshl_b32 s58, s3, 13\n"
      "s_add_u32 s58, 0xf6000, s58\n"
      "s_bfe_u32 s57, s41, 0x30003\n"
      "s_lshl_b32 s57, s57, 6\n"
      "s_add_u32 s57, s58, s57\n"
      "s_bfe_u32 s58, s41, 0x20001\n"
      "s_lshl_b32 s58, s58, 4\n"
      "s_add_u32 s58, s57, s58\n"
      "s_lshr_b32 s57, s41, 6\n"
      "s_lshl_b32 s57, s57, 3\n"
      "s_add_u32 s57, s58, s57\n"
      "v_lshl_add_u32 v30, v2, 9, s57\n"
      "s_lshl_b32 s57, s39, 12\n"
      "s_add_u32 s57, 0xfe000, s57\n"
      "s_lshl_b32 s58, s40, 7\n"
      "s_add_u32 s58, s57, s58\n"
      "v_bfe_u32 v5, v0, 3, 2\n"
      "v_lshl_add_u32 v5, v5, 10, s58\n"
      "v_lshrrev_b32 v7, 5, v0\n"
      "v_lshl_add_u32 v5, v7, 9, v5\n"
      "v_and_b32 v7, 7, v0\n"
      "v_lshl_add_u32 v14, v7, 4, v5\n"
      "v_mov_b32 v22, 1.0\n"
      "s_nop 1\n"
      "s_setpc_b64 s[0:1]\n"
      ".Lfd_sub_t:\n"
      "s_nop 7\n"
      "s_lshl_b32 s58, s36, 12\n"
      "s_add_u32 s46, s45, s58\n"
      "v_lshl_add_u32 v5, v1, 3, v3\n"
      "v_lshl_add_u32 v5, v5, 7, s46\n"
      "v_lshl_add_u32 v13, v4, 3, v5\n"
      "v_lshrrev_b32 v5, 5, v0\n"
      "v_lshlrev_b32 v5, 6, v5\n"
      "v_add_u32 v5, 0x27980, v5\n"
      "v_and_b32 v7, 7, v0\n"
      "v_lshl_add_u32 v5, v7, 3, v5\n"
      "v_and_b32 v7, 1, v1\n"
      "v_lshl_add_u32 v15, v7, 2, v5\n"
      "v_and_b32 v5, 1, v0\n"
      "v_lshlrev_b32 v5, 6, v5\n"
      "v_add_u32 v5, 0x27980, v5\n"
      "v_lshl_add_u32 v16, v1, 4, v5\n"
      "s_mul_i32 s58, s37, 36\n"
      "s_lshl_b32 s57, s36, 2\n"
      "s_add_u32 s57, s58, s57\n"
      "s_mul_i32 s57, s57, 0x4080\n"
      "s_add_u32 s57, 0x182000, s57\n"
      "s_lshl_b32 s58, s38, 7\n"
      "s_add_u32 s58, s57, s58\n"
      "v_mul_u32_u24 v5, 0x4080, v1\n"
      "v_add_u32 v5, s58, v5\n"
      "v_lshl_add_u32 v17, v2, 3, v5\n"
      "s_mul_i32 s58, s37, 36\n"
      "s_add_u32 s58, s58, s38\n"
      "s_mul_i32 s58, s58, 0x4080\n"
      "s_add_u32 s58, 0x182000, s58\n"
      "s_lshl_b32 s57, s36, 11\n"
      "s_add_u32 s57, s58, s57\n"
      "v_lshl_add_u32 v18, v0, 4, s57\n"
      "s_lshl_b32 s57, s36, 7\n"
      "s_add_u32 s57, 0x27a00, s57\n"
      "v_lshrrev_b32 v5, 5, v0\n"
      "v_lshl_add_u32 v5, v5, 6, s57\n"
      "v_and_b32 v7, 7, v0\n"
      "v_lshl_add_u32 v5, v7, 3, v5\n"
      "v_and_b32 v7, 1, v1\n"
      "v_lshl_add_u32 v19, v7, 2, v5\n"
      "v_and_b32 v5, 31, v0\n"
      "v_lshlrev_b32 v5, 2, v5\n"
      "v_add_u32 v20, 0x27a00, v5\n"
      "s_mul_i32 s57, s37, 0x240\n"
      "s_lshl_b32 s58, s38, 4\n"
      "s_add_u32 s58, s57, s58\n"
      "s_lshl_b32 s58, s58, 1\n"
      "v_and_b32 v5, 1, v1\n"
      "v_mul_u32_u24 v5, 0x900, v5\n"
      "v_add_u32 v5, s58, v5\n"
      "v_lshl_add_u32 v21, v2, 1, v5\n"
      "v_lshrrev_b32 v5, 3, v0\n"
      "v_mul_u32_u24 v5, 0x480, v5\n"
      "v_and_b32 v7, 7, v0\n"
      "v_lshl_add_u32 v24, v7, 4, v5\n"
      "s_mul_i32 s58, s38, 0x9000\n"
      "s_lshl_b32 s57, s36, 7\n"
      "s_add_u32 s63, s58, s57\n"
      "s_lshl_b32 s57, s37, 17\n"
      "s_add_u32 s57, 0xb1000, s57\n"
      "s_lshr_b32 s58, s38, 4\n"
      "s_lshl_b32 s58, s58, 14\n"
      "s_add_u32 s58, s57, s58\n"
      "s_bfe_u32 s57, s38, 0x20002\n"
      "s_lshl_b32 s57, s57, 6\n"
      "s_add_u32 s57, s58, s57\n"
      "s_and_b32 s58, s38, 3\n"
      "s_lshl_b32 s58, s58, 2\n"
      "s_add_u32 s58, s57, s58\n"
      "v_lshl_add_u32 v25, v0, 8, s58\n"
      "s_nop 1\n"
      "s_setpc_b64 s[0:1]\n"
      ".Lfd_sub_cf:\n"
      "ds_read_b64_tr_b16 v[120:121], v248 offset:0\n"
      "ds_read_b64_tr_b16 v[136:137], v249 offset:0\n"
      "ds_read_b64_tr_b16 v[152:153], v250 offset:0\n"
      "ds_read_b64_tr_b16 v[168:169], v251 offset:0\n"
      "ds_read_b64_tr_b16 v[122:123], v248 offset:4096\n"
      "ds_read_b64_tr_b16 v[138:139], v249 offset:4096\n"
      "ds_read_b64_tr_b16 v[154:155], v250 offset:4096\n"
      "ds_read_b64_tr_b16 v[170:171], v251 offset:4096\n"
      "ds_read_b64_tr_b16 v[124:125], v248 offset:8192\n"
      "ds_read_b64_tr_b16 v[140:141], v249 offset:8192\n"
      "ds_read_b64_tr_b16 v[156:157], v250 offset:8192\n"
      "ds_read_b64_tr_b16 v[172:173], v251 offset:8192\n"
      "ds_read_b64_tr_b16 v[126:127], v248 offset:12288\n"
      "ds_read_b64_tr_b16 v[142:143], v249 offset:12288\n"
      "ds_read_b64_tr_b16 v[158:159], v250 offset:12288\n"
      "ds_read_b64_tr_b16 v[174:175], v251 offset:12288\n"
      "ds_read_b64_tr_b16 v[128:129], v248 offset:16384\n"
      "ds_read_b64_tr_b16 v[144:145], v249 offset:16384\n"
      "ds_read_b64_tr_b16 v[160:161], v250 offset:16384\n"
      "ds_read_b64_tr_b16 v[176:177], v251 offset:16384\n"
      "ds_read_b64_tr_b16 v[130:131], v248 offset:20480\n"
      "ds_read_b64_tr_b16 v[146:147], v249 offset:20480\n"
      "ds_read_b64_tr_b16 v[162:163], v250 offset:20480\n"
      "ds_read_b64_tr_b16 v[178:179], v251 offset:20480\n"
      "s_setpc_b64 s[0:1]\n"
      ".Lfd_sub_a1:\n"
      "ds_read_b128 v[184:187], v112 offset:0\n"
      "ds_read_b128 v[188:191], v113 offset:0\n"
      "ds_read_b128 v[192:195], v114 offset:0\n"
      "ds_read_b128 v[196:199], v115 offset:0\n"
      "ds_read_b128 v[200:203], v112 offset:32768\n"
      "v_add_u32 v112, 0x10000, v112\n"
      "ds_read_b128 v[204:207], v113 offset:32768\n"
      "v_add_u32 v113, 0x10000, v113\n"
      "ds_read_b128 v[208:211], v114 offset:32768\n"
      "v_add_u32 v114, 0x10000, v114\n"
      "ds_read_b128 v[212:215], v115 offset:32768\n"
      "v_add_u32 v115, 0x10000, v115\n"
      "ds_read_b128 v[216:219], v112 offset:0\n"
      "ds_read_b128 v[220:223], v113 offset:0\n"
      "ds_read_b128 v[224:227], v114 offset:0\n"
      "ds_read_b128 v[228:231], v115 offset:0\n"
      "ds_read_b128 v[232:235], v112 offset:32768\n"
      "ds_read_b128 v[236:239], v113 offset:32768\n"
      "ds_read_b128 v[240:243], v114 offset:32768\n"
      "ds_read_b128 v[244:247], v115 offset:32768\n"
      "s_setpc_b64 s[0:1]\n"
      ".Lfd_roleA:\n"
      "s_cmp_lg_u32 s36, 2\n"
      "s_cbranch_scc1 .Lfd_x_26\n"
      "v_cmp_lt_u32 vcc, 17, v0\n"
      "v_mov_b32 v5, 864\n"
      "s_nop 0\n"
      "v_cndmask_b32 v5, 0, v5, vcc\n"
      "v_add_u32 v5, v5, v11\n"
      "s_mul_i32 s55, s40, 0x120\n"
      "s_mov_b32 m0, 160256\n"
      "s_mov_b32 exec_hi, 15\n"
      "buffer_load_dwordx4 v5, s[8:11], s55 offen lds\n"
      "s_mov_b32 exec_hi, -1\n"
      "buffer_load_dword v12, off, s[12:15], 0 sc1\n"
      ".Lfd_x_26:\n"
      "v_bfe_u32 v3, v0, 2, 2\n"
      "v_and_b32 v4, 3, v0\n"
      "s_mov_b32 s47, 0\n"
      "s_mov_b32 s48, 0xffff0000\n"
      "s_mov_b32 s49, 0xffff0000\n"
      "s_mov_b32 s50, 0\n"
      "s_mov_b32 s51, -1\n"
      "v_mul_u32_u24 v5, 0x480, v1\n"
      "v_add_u32 v5, s46, v5\n"
      "v_lshl_add_u32 v5, v3, 8, v5\n"
      "s_lshl_b32 s58, s36, 1\n"
      "v_lshrrev_b32 v7, 1, v4\n"
      "v_add_u32 v7, s58, v7\n"
      "v_lshlrev_b32 v6, 1, v3\n"
      "v_xor_b32 v6, v7, v6\n"
      "v_lshl_add_u32 v5, v6, 4, v5\n"
      "v_and_b32 v6, 1, v4\n"
      "v_lshl_add_u32 v28, v6, 3, v5\n"
      "v_and_b32 v5, 1, v0\n"
      "v_mul_u32_u24 v5, 0x120, v5\n"
      "v_add_u32 v5, 0x27200, v5\n"
      "v_lshl_add_u32 v29, v1, 3, v5\n"
      "s_lshl_b32 s58, s37, 7\n"
      "s_add_u32 s58, s58, s38\n"
      "s_lshl_b32 s58, s58, 10\n"
      "s_add_u32 s58, 0x1000, s58\n"
      "s_lshl_b32 s57, s36, 6\n"
      "s_add_u32 s57, s58, s57\n"
      "v_lshl_add_u32 v5, v1, 9, s57\n"
      "v_lshl_add_u32 v30, v2, 2, v5\n"
      "v_lshlrev_b32 v5, 8, v1\n"
      "v_xor_b32 v6, v2, v1\n"
      "v_lshl_add_u32 v34, v6, 4, v5\n"
      "s_mul_i32 s41, s36, 0x3600\n"
      "s_bfe_u32 s57, s38, 0x10002\n"
      "s_lshl_b32 s57, s57, 4\n"
      "s_lshr_b32 s58, s38, 3\n"
      "s_add_u32 s58, s57, s58\n"
      "s_lshl_b32 s58, s58, 13\n"
      "s_add_u32 s58, s58, 0x1800\n"
      "s_lshl_b32 s57, s40, 2\n"
      "s_add_u32 s57, s58, s57\n"
      "s_and_b32 s58, s36, 1\n"
      "s_lshl_b32 s58, s58, 6\n"
      "s_add_u32 s58, s57, s58\n"
      "s_lshr_b32 s57, s36, 1\n"
      "s_lshl_b32 s57, s57, 7\n"
      "s_add_u32 s57, s58, s57\n"
      "s_lshl_b32 s3, s57, 8\n"
      "s_cmp_lg_u32 s36, 3\n"
      "s_cbranch_scc1 .Lfd_pt_27\n"
      "s_mov_b32 s76, s3\n"
      "s_buffer_load_dword s76, s[16:19], s76\n"
      ".Lfd_pt_27:\n"
      "v_mov_b32 v42, 0x27900\n"
      "v_mov_b32 v6, 0\n"
      "v_mov_b32 v7, 0\n"
      "ds_write_b64 v42, v[6:7]\n"
      "s_add_u32 s52, s52, 262144\n"
      "s_add_u32 m0, s53, 18432\n"
      "s_waitcnt vmcnt(1)\n"
      "buffer_load_dwordx4 v26, s[4:7], s52 offen nt lds\n"
      "s_add_u32 s52, s52, 262144\n"
      "s_add_u32 m0, s53, 27648\n"
      "s_waitcnt vmcnt(1)\n"
      "buffer_load_dwordx4 v26, s[4:7], s52 offen nt lds\n"
      "s_lshr_b32 s57, s36, 1\n"
      "s_add_u32 s57, 32, s57\n"
      "s_lshl_b32 s57, s57, 15\n"
      "s_add_u32 s57, s54, s57\n"
      "s_and_b32 s58, s36, 1\n"
      "s_lshl_b32 s58, s58, 14\n"
      "s_add_u32 s52, s57, s58\n"
      "s_lshr_b32 s58, s36, 1\n"
      "s_add_u32 s58, 32, s58\n"
      "s_mul_i32 s58, s58, 0x480\n"
      "s_add_u32 s58, s46, s58\n"
      "s_and_b32 s57, s36, 1\n"
      "s_lshl_b32 s57, s57, 9\n"
      "s_add_u32 m0, s58, s57\n"
      "s_and_b32 s57, s36, 1\n"
      "s_lshl_b32 s57, s57, 6\n"
      "v_xor_b32 v27, s57, v26\n"
      "s_waitcnt vmcnt(1)\n"
      "s_mov_b32 exec_hi, 0\n"
      "buffer_load_dwordx4 v27, s[4:7], s52 offen nt lds\n"
      "s_mov_b32 exec_hi, -1\n"
      "s_lshr_b32 s57, s36, 1\n"
      "s_add_u32 s57, 0, s57\n"
      "s_and_b32 s57, s57, 3\n"
      "s_lshl_b32 s57, s57, 12\n"
      "s_add_u32 s90, s3, s57\n"
      "s_lshr_b32 s57, s36, 1\n"
      "s_add_u32 s57, 1, s57\n"
      "s_and_b32 s57, s57, 3\n"
      "s_lshl_b32 s57, s57, 12\n"
      "s_add_u32 s91, s3, s57\n"
      "s_lshr_b32 s57, s36, 1\n"
      "s_add_u32 s57, 2, s57\n"
      "s_and_b32 s57, s57, 3\n"
      "s_lshl_b32 s57, s57, 12\n"
      "s_add_u32 s92, s3, s57\n"
      "s_lshr_b32 s57, s36, 1\n"
      "s_add_u32 s57, 3, s57\n"
      "s_and_b32 s57, s57, 3\n"
      "s_lshl_b32 s57, s57, 12\n"
      "s_add_u32 s93, s3, s57\n"
      "s_add_u32 s44, s90, 0\n"
      "s_add_u32 m0, s41, 0\n"
      "s_waitcnt vmcnt(1)\n"
      "buffer_load_dwordx4 v34, s[16:19], s44 offen nt lds\n"
      "s_add_u32 s44, s91, 0\n"
      "s_add_u32 m0, s41, 1088\n"
      "s_waitcnt vmcnt(1)\n"
      "buffer_load_dwordx4 v34, s[16:19], s44 offen nt lds\n"
      "v_mov_b32 v25, 162064\n"
      "s_cmp_lg_u32 s36, 2\n"
      "s_cbranch_scc1 .Lfd_e_28\n"
      "ds_write_b32 v25, v12\n"
      ".Lfd_e_28:\n"
      "s_waitcnt lgkmcnt(0)\n"
      "s_barrier\n"
      "ds_read_b32 v12, v25\n"
      "ds_read2_b64 v[72:75], v29 offset0:0 offset1:4\n"
      "ds_read_b64_tr_b16 v[90:91], v28 offset:0\n"
      "ds_read_b64_tr_b16 v[92:93], v28 offset:4608\n"
      "ds_read2_b64 v[76:79], v29 offset0:8 offset1:12\n"
      "ds_read_b64_tr_b16 v[94:95], v28 offset:9216\n"
      "ds_read_b64_tr_b16 v[96:97], v28 offset:13824\n"
      "ds_read2_b64 v[80:83], v29 offset0:16 offset1:20\n"
      "ds_read_b64_tr_b16 v[98:99], v28 offset:18432\n"
      "ds_read_b64_tr_b16 v[100:101], v28 offset:23040\n"
      "ds_read2_b64 v[84:87], v29 offset0:24 offset1:28\n"
      "ds_read_b64_tr_b16 v[102:103], v28 offset:27648\n"
      "ds_read_b64_tr_b16 v[104:105], v28 offset:32256\n"
      "ds_read_b64 v[88:89], v29 offset:256\n"
      "ds_read_b64_tr_b16 v[106:107], v28 offset:36864\n"
      "s_waitcnt lgkmcnt(11)\n"
      "v_mfma_f32_16x16x32_bf16 v[108:111], v[72:75], v[90:93], 0\n"
      "s_waitcnt lgkmcnt(8)\n"
      "v_mfma_f32_16x16x32_bf16 v[112:115], v[76:79], v[94:97], 0\n"
      "s_waitcnt lgkmcnt(5)\n"
      "v_mfma_f32_16x16x32_bf16 v[108:111], v[80:83], v[98:101], v[108:111]\n"
      "s_waitcnt lgkmcnt(2)\n"
      "v_mfma_f32_16x16x32_bf16 v[112:115], v[84:87], v[102:105], v[112:115]\n"
      "s_waitcnt lgkmcnt(0)\n"
      "v_mfma_f32_16x16x16_bf16 v[116:119], v[88:89], v[106:107], 0\n"
      "v_readfirstlane_b32 s43, v12\n"
      "s_add_u32 s43, s43, 1\n"
      "s_mov_b32 s42, s43\n"
      "v_mov_b32 v10, s43\n"
      "v_mov_b32 v33, s43\n"
      "s_nop 0\n"
      "v_pk_add_f32 v[108:109], v[108:109], v[112:113]\n"
      "s_nop 0\n"
      "v_pk_add_f32 v[108:109], v[108:109], v[116:117]\n"
      "v_cndmask_b32 v108, v108, v109, s[48:49]\n"
      "s_nop 1\n"
      "v_mov_b32_dpp v5, v108 quad_perm:[1,0,3,2] row_mask:0xf bank_mask:0xf\n"
      "v_cvt_pk_bf16_f32 v32, v108, v5\n"
      "s_mov_b32 exec_lo, 0x55555555\n"
      "s_mov_b32 exec_hi, 0\n"
      "buffer_store_dwordx2 v[32:33], v30, s[12:15], 0 offen sc1\n"
      "s_mov_b64 exec, -1\n"
      "s_add_u32 s44, s92, 0\n"
      "s_add_u32 m0, s41, 2176\n"
      "s_waitcnt vmcnt(2) lgkmcnt(0)\n"
      "buffer_load_dwordx4 v34, s[16:19], s44 offen nt lds\n"
      "s_cmp_lg_u32 s36, 3\n"
      "s_cbranch_scc1 .Lfd_pt_29\n"
      "s_mov_b32 s84, s3\n"
      "s_buffer_load_dword s84, s[20:23], s84\n"
      "s_mul_i32 s85, s38, 0x9000\n"
      "s_buffer_load_dword s85, s[28:31], s85\n"
      "s_mov_b32 s86, 0\n"
      "s_buffer_load_dword s86, s[32:35], s86\n"
      "s_mov_b32 s87, 0x2a4380\n"
      "s_buffer_load_dword s87, s[12:15], s87\n"
      "s_mov_b32 s88, 0x4ac3fc\n"
      "s_buffer_load_dword s88, s[12:15], s88\n"
      ".Lfd_pt_29:\n"
      "v_lshlrev_b32 v5, 8, v1\n"
      "v_lshlrev_b32 v6, 1, v1\n"
      "v_xor_b32 v6, v2, v6\n"
      "v_lshl_add_u32 v35, v6, 4, v5\n"
      "v_mul_u32_u24 v5, 0x440, v3\n"
      "v_add_u32 v5, s41, v5\n"
      "v_lshl_add_u32 v5, v4, 8, v5\n"
      "v_xor_b32 v6, v1, v4\n"
      "v_lshl_add_u32 v36, v6, 4, v5\n"
      "v_mul_u32_u24 v5, 0x480, v1\n"
      "v_add_u32 v5, s41, v5\n"
      "v_lshl_add_u32 v5, v3, 8, v5\n"
      "v_and_b32 v6, 1, v4\n"
      "v_lshl_add_u32 v5, v6, 3, v5\n"
      "v_lshrrev_b32 v6, 1, v4\n"
      "v_lshlrev_b32 v7, 1, v3\n"
      "v_xor_b32 v7, v6, v7\n"
      "v_lshl_add_u32 v50, v7, 4, v5\n"
      "v_xor_b32 v51, 32, v50\n"
      "v_xor_b32 v52, 64, v50\n"
      "v_xor_b32 v53, 96, v50\n"
      "s_lshl_b32 s57, s36, 8\n"
      "s_add_u32 s57, 0x25200, s57\n"
      "v_and_b32 v5, 31, v0\n"
      "v_lshl_add_u32 v38, v5, 3, s57\n"
      "v_lshrrev_b32 v5, 5, v0\n"
      "v_lshlrev_b32 v5, 10, v5\n"
      "v_add_u32 v5, 0x25200, v5\n"
      "v_and_b32 v7, 31, v0\n"
      "v_lshl_add_u32 v165, v7, 3, v5\n"
      "v_lshlrev_b32 v5, 3, v0\n"
      "v_add_u32 v40, 0x27480, v5\n"
      "v_and_b32 v5, 1, v0\n"
      "v_lshlrev_b32 v5, 8, v5\n"
      "v_add_u32 v5, 0x27480, v5\n"
      "v_lshl_add_u32 v41, v1, 4, v5\n"
      "s_lshl_b32 s57, s36, 4\n"
      "s_add_u32 s57, 0x27880, s57\n"
      "v_and_b32 v5, 1, v0\n"
      "v_lshl_add_u32 v43, v5, 3, s57\n"
      "v_lshlrev_b32 v5, 3, v2\n"
      "v_add_u32 v44, 0x27880, v5\n"
      "s_lshr_b32 s57, s38, 3\n"
      "s_lshl_b32 s57, s57, 9\n"
      "s_add_u32 s57, 0xf2000, s57\n"
      "s_and_b32 s58, s38, 7\n"
      "s_lshl_b32 s58, s58, 2\n"
      "s_add_u32 s58, s57, s58\n"
      "v_lshl_add_u32 v164, v0, 6, s58\n"
      "s_and_b32 s58, s38, -8\n"
      "s_add_u32 s58, s58, s36\n"
      "s_lshl_b32 s58, s58, 10\n"
      "s_add_u32 s58, 0x1000, s58\n"
      "s_bfe_u32 s57, s38, 0x10002\n"
      "s_lshl_b32 s57, s57, 9\n"
      "s_add_u32 s57, s58, s57\n"
      "s_add_u32 s57, s57, 0x1c0\n"
      "s_add_u32 s65, s57, 4\n"
      "s_lshl_b32 s57, s36, 10\n"
      "s_add_u32 s57, 0x25200, s57\n"
      "v_lshl_add_u32 v45, v2, 3, s57\n"
      "s_and_b32 s57, s36, 1\n"
      "s_lshl_b32 s57, s57, 9\n"
      "s_add_u32 s57, 0x25200, s57\n"
      "s_bfe_u32 s58, s36, 0x10001\n"
      "s_lshl_b32 s58, s58, 2\n"
      "s_add_u32 s58, s57, s58\n"
      "v_lshl_add_u32 v46, v0, 3, s58\n"
      "s_lshl_b32 s58, s38, 10\n"
      "s_add_u32 s58, 0x13e000, s58\n"
      "s_lshl_b32 s57, s36, 8\n"
      "s_add_u32 s57, s58, s57\n"
      "v_lshl_add_u32 v47, v0, 2, s57\n"
      "s_lshl_b32 s57, s38, 7\n"
      "s_add_u32 s57, 0x17e000, s57\n"
      "v_lshl_add_u32 v5, v0, 4, s57\n"
      "s_lshl_b32 s57, s36, 6\n"
      "v_add_u32 v7, s57, v0\n"
      "v_lshrrev_b32 v7, 1, v7\n"
      "v_lshl_add_u32 v48, v7, 24, v5\n"
      "s_and_b32 s57, s38, -8\n"
      "s_lshl_b32 s57, s57, 7\n"
      "s_add_u32 s57, 0x17e000, s57\n"
      "s_and_b32 s58, s39, 1\n"
      "s_lshl_b32 s58, s58, 4\n"
      "s_add_u32 s58, s57, s58\n"
      "s_xor_b32 s57, s36, 4\n"
      "s_lshl_b32 s57, s57, 25\n"
      "s_add_u32 s57, s58, s57\n"
      "v_lshrrev_b32 v5, 3, v0\n"
      "v_lshl_add_u32 v49, v5, 7, s57\n"
      "s_and_b32 s57, s38, -8\n"
      "s_lshl_b32 s57, s57, 10\n"
      "s_add_u32 s57, 0x13e000, s57\n"
      "s_and_b32 s58, s39, 1\n"
      "s_lshl_b32 s58, s58, 9\n"
      "s_add_u32 s58, s57, s58\n"
      "s_lshl_b32 s57, s40, 7\n"
      "s_add_u32 s57, s58, s57\n"
      "v_lshrrev_b32 v5, 3, v0\n"
      "v_lshl_add_u32 v5, v5, 10, s57\n"
      "v_and_b32 v7, 7, v0\n"
      "v_lshl_add_u32 v14, v7, 4, v5\n"
      "s_mov_b32 s64, 0x3e0293ee\n"
      "v_mov_b32 v55, 1\n"
      "s_call_b64 s[0:1], .Lfd_sub_t\n"
      "s_and_b32 s57, s38, -8\n"
      "s_add_u32 s57, s57, s36\n"
      "s_lshl_b32 s57, s57, 10\n"
      "s_add_u32 s57, 0x1000, s57\n"
      "s_bfe_u32 s58, s38, 0x10002\n"
      "s_lshl_b32 s58, s58, 9\n"
      "s_add_u32 s58, s57, s58\n"
      "v_and_b32 v5, 31, v0\n"
      "v_lshl_add_u32 v37, v5, 4, s58\n"
      "s_add_u32 s44, s93, 0\n"
      "s_add_u32 m0, s41, 3264\n"
      "s_waitcnt vmcnt(2) lgkmcnt(0)\n"
      "buffer_load_dwordx4 v34, s[16:19], s44 offen nt lds\n"
      "s_add_u32 s44, s90, 131072\n"
      "s_add_u32 m0, s41, 4608\n"
      "s_waitcnt vmcnt(1) lgkmcnt(0)\n"
      "s_mov_b32 s61, 0\n"
      ".Lfd_p_30:\n"
      "s_buffer_load_dword s68, s[12:15], s65 glc\n"
      "s_waitcnt lgkmcnt(0)\n"
      "s_cmp_eq_u32 s68, s43\n"
      "s_cbranch_scc0 .Lfd_r_31\n"
      ".Lfd_d_32:\n"
      "buffer_load_dwordx4 v[144:147], v37, s[12:15], 0 offen sc1\n"
      "buffer_load_dwordx4 v34, s[16:19], s44 offen nt lds\n"
      "s_add_u32 s44, s91, 131072\n"
      "s_add_u32 m0, s41, 5696\n"
      "s_waitcnt vmcnt(2) lgkmcnt(0)\n"
      "buffer_load_dwordx4 v34, s[16:19], s44 offen nt lds\n"
      "s_waitcnt vmcnt(2)\n"
      "v_bitop3_b32 v5, v145, v147, s43 bitop3:0x7e\n"
      "v_cmp_ne_u32 vcc, 0, v5\n"
      "s_cbranch_vccnz .Lfd_r_34\n"
      ".Lfd_d_35:\n"
      "ds_write2_b32 v38, v144, v146 offset1:1\n"
      "s_mov_b32 exec_lo, 1\n"
      "s_mov_b32 exec_hi, 0\n"
      "ds_add_u32 v42, v55\n"
      "s_mov_b64 exec, -1\n"
      "s_add_u32 s44, s92, 131072\n"
      "s_add_u32 m0, s41, 6784\n"
      "s_waitcnt vmcnt(1) lgkmcnt(0)\n"
      "buffer_load_dwordx4 v34, s[16:19], s44 offen nt lds\n"
      "s_add_u32 s44, s93, 131072\n"
      "s_add_u32 m0, s41, 7872\n"
      "s_waitcnt vmcnt(1) lgkmcnt(0)\n"
      "buffer_load_dwordx4 v34, s[16:19], s44 offen nt lds\n"
      ".Lfd_s_36:\n"
      "ds_read_b32 v5, v42 offset:0\n"
      "s_waitcnt lgkmcnt(0)\n"
      "v_cmp_ne_u32 vcc, 8, v5\n"
      "s_cbranch_vccnz .Lfd_s_36\n"
      "ds_read_b64 v[128:129], v165 offset:0\n"
      "ds_read_b64 v[130:131], v165 offset:256\n"
      "ds_read_b64 v[132:133], v165 offset:512\n"
      "ds_read_b64 v[134:135], v165 offset:768\n"
      "s_waitcnt lgkmcnt(0)\n"
      "v_lshlrev_b32 v136, 16, v128\n"
      "v_and_b32 v137, 0xffff0000, v128\n"
      "v_lshlrev_b32 v138, 16, v129\n"
      "v_and_b32 v139, 0xffff0000, v129\n"
      "v_lshlrev_b32 v140, 16, v130\n"
      "v_and_b32 v141, 0xffff0000, v130\n"
      "v_lshlrev_b32 v142, 16, v131\n"
      "v_and_b32 v143, 0xffff0000, v131\n"
      "v_pk_add_f32 v[136:137], v[136:137], v[140:141]\n"
      "v_pk_add_f32 v[138:139], v[138:139], v[142:143]\n"
      "v_lshlrev_b32 v140, 16, v132\n"
      "v_and_b32 v141, 0xffff0000, v132\n"
      "v_lshlrev_b32 v142, 16, v133\n"
      "v_and_b32 v143, 0xffff0000, v133\n"
      "v_pk_add_f32 v[136:137], v[136:137], v[140:141]\n"
      "v_pk_add_f32 v[138:139], v[138:139], v[142:143]\n"
      "v_lshlrev_b32 v140, 16, v134\n"
      "v_and_b32 v141, 0xffff0000, v134\n"
      "v_lshlrev_b32 v142, 16, v135\n"
      "v_and_b32 v143, 0xffff0000, v135\n"
      "v_pk_add_f32 v[136:137], v[136:137], v[140:141]\n"
      "v_pk_add_f32 v[138:139], v[138:139], v[142:143]\n"
      "v_cvt_pk_bf16_f32 v136, v136, v137\n"
      "v_cvt_pk_bf16_f32 v137, v138, v139\n"
      "ds_write_b64 v40, v[136:137]\n"
      "ds_read_b128 v[56:59], v41 offset:0\n"
      "ds_read_b128 v[60:63], v41 offset:64\n"
      "ds_read_b128 v[64:67], v41 offset:128\n"
      "ds_read_b128 v[68:71], v41 offset:192\n"
      "s_waitcnt lgkmcnt(0)\n"
      "ds_read_b128 v[128:131], v36 offset:0\n"
      "ds_read_b128 v[132:135], v36 offset:64\n"
      "ds_read_b128 v[136:139], v36 offset:128\n"
      "ds_read_b128 v[140:143], v36 offset:192\n"
      "s_waitcnt lgkmcnt(3)\n"
      "v_mfma_f32_16x16x32_bf16 v[72:75], v[128:131], v[56:59], 0\n"
      "s_waitcnt lgkmcnt(2)\n"
      "v_mfma_f32_16x16x32_bf16 v[72:75], v[132:135], v[60:63], v[72:75]\n"
      "s_waitcnt lgkmcnt(1)\n"
      "v_mfma_f32_16x16x32_bf16 v[72:75], v[136:139], v[64:67], v[72:75]\n"
      "s_waitcnt lgkmcnt(0)\n"
      "v_mfma_f32_16x16x32_bf16 v[72:75], v[140:143], v[68:71], v[72:75]\n"
      "s_add_u32 s44, s90, 262144\n"
      "s_add_u32 m0, s41, 9216\n"
      "s_waitcnt vmcnt(1) lgkmcnt(0)\n"
      "buffer_load_dwordx4 v34, s[16:19], s44 offen nt lds\n"
      "s_add_u32 s44, s91, 262144\n"
      "s_add_u32 m0, s41, 10304\n"
      "s_waitcnt vmcnt(3) lgkmcnt(0)\n"
      "buffer_load_dwordx4 v34, s[16:19], s44 offen nt lds\n"
      "s_add_u32 s44, s92, 262144\n"
      "s_add_u32 m0, s41, 11392\n"
      "s_waitcnt vmcnt(3) lgkmcnt(0)\n"
      "buffer_load_dwordx4 v34, s[16:19], s44 offen nt lds\n"
      "s_add_u32 s44, s93, 262144\n"
      "s_add_u32 m0, s41, 12480\n"
      "s_waitcnt vmcnt(3) lgkmcnt(0)\n"
      "buffer_load_dwordx4 v34, s[16:19], s44 offen nt lds\n"
      "ds_read_b128 v[128:131], v36 offset:4608\n"
      "ds_read_b128 v[132:135], v36 offset:4672\n"
      "ds_read_b128 v[136:139], v36 offset:4736\n"
      "ds_read_b128 v[140:143], v36 offset:4800\n"
      "s_waitcnt lgkmcnt(3)\n"
      "v_mfma_f32_16x16x32_bf16 v[76:79], v[128:131], v[56:59], 0\n"
      "s_waitcnt lgkmcnt(2)\n"
      "v_mfma_f32_16x16x32_bf16 v[76:79], v[132:135], v[60:63], v[76:79]\n"
      "s_waitcnt lgkmcnt(1)\n"
      "v_mfma_f32_16x16x32_bf16 v[76:79], v[136:139], v[64:67], v[76:79]\n"
      "s_waitcnt lgkmcnt(0)\n"
      "v_mfma_f32_16x16x32_bf16 v[76:79], v[140:143], v[68:71], v[76:79]\n"
      "s_add_u32 s44, s90, 393216\n"
      "s_add_u32 m0, s41, 0\n"
      "s_waitcnt vmcnt(3) lgkmcnt(0)\n"
      "buffer_load_dwordx4 v34, s[16:19], s44 offen nt lds\n"
      "s_add_u32 s44, s91, 393216\n"
      "s_add_u32 m0, s41, 1088\n"
      "s_waitcnt vmcnt(3) lgkmcnt(0)\n"
      "buffer_load_dwordx4 v34, s[16:19], s44 offen nt lds\n"
      "s_add_u32 s44, s92, 393216\n"
      "s_add_u32 m0, s41, 2176\n"
      "s_waitcnt vmcnt(3) lgkmcnt(0)\n"
      "buffer_load_dwordx4 v34, s[16:19], s44 offen nt lds\n"
      "s_add_u32 s44, s93, 393216\n"
      "s_add_u32 m0, s41, 3264\n"
      "s_waitcnt vmcnt(3) lgkmcnt(0)\n"
      "buffer_load_dwordx4 v34, s[16:19], s44 offen nt lds\n"
      "ds_read_b128 v[128:131], v36 offset:9216\n"
      "ds_read_b128 v[132:135], v36 offset:9280\n"
      "ds_read_b128 v[136:139], v36 offset:9344\n"
      "ds_read_b128 v[140:143], v36 offset:9408\n"
      "s_waitcnt lgkmcnt(3)\n"
      "v_mfma_f32_16x16x32_bf16 v[80:83], v[128:131], v[56:59], 0\n"
      "s_waitcnt lgkmcnt(2)\n"
      "v_mfma_f32_16x16x32_bf16 v[80:83], v[132:135], v[60:63], v[80:83]\n"
      "s_waitcnt lgkmcnt(1)\n"
      "v_mfma_f32_16x16x32_bf16 v[80:83], v[136:139], v[64:67], v[80:83]\n"
      "s_waitcnt lgkmcnt(0)\n"
      "v_mfma_f32_16x16x32_bf16 v[80:83], v[140:143], v[68:71], v[80:83]\n"
      "s_add_u32 s44, s90, 0\n"
      "s_add_u32 m0, s41, 4608\n"
      "s_waitcnt vmcnt(3) lgkmcnt(0)\n"
      "buffer_load_dwordx4 v35, s[20:23], s44 offen nt lds\n"
      "s_add_u32 s44, s91, 0\n"
      "s_add_u32 m0, s41, 5760\n"
      "s_waitcnt vmcnt(3) lgkmcnt(0)\n"
      "buffer_load_dwordx4 v35, s[20:23], s44 offen nt lds\n"
      "s_add_u32 s44, s92, 0\n"
      "s_add_u32 m0, s41, 6912\n"
      "s_waitcnt vmcnt(3) lgkmcnt(0)\n"
      "buffer_load_dwordx4 v35, s[20:23], s44 offen nt lds\n"
      "s_add_u32 s44, s93, 0\n"
      "s_add_u32 m0, s41, 8064\n"
      "s_waitcnt vmcnt(3) lgkmcnt(0)\n"
      "buffer_load_dwordx4 v35, s[20:23], s44 offen nt lds\n"
      "ds_read_b128 v[128:131], v36 offset:0\n"
      "ds_read_b128 v[132:135], v36 offset:64\n"
      "ds_read_b128 v[136:139], v36 offset:128\n"
      "ds_read_b128 v[140:143], v36 offset:192\n"
      "s_waitcnt lgkmcnt(3)\n"
      "v_mfma_f32_16x16x32_bf16 v[84:87], v[128:131], v[56:59], 0\n"
      "s_waitcnt lgkmcnt(2)\n"
      "v_mfma_f32_16x16x32_bf16 v[84:87], v[132:135], v[60:63], v[84:87]\n"
      "s_waitcnt lgkmcnt(1)\n"
      "v_mfma_f32_16x16x32_bf16 v[84:87], v[136:139], v[64:67], v[84:87]\n"
      "s_waitcnt lgkmcnt(0)\n"
      "v_mfma_f32_16x16x32_bf16 v[84:87], v[140:143], v[68:71], v[84:87]\n"
      "v_max3_f32 v5, v72, v73, v74\n"
      "v_max3_f32 v6, v75, v76, v77\n"
      "v_max3_f32 v7, v78, v79, v80\n"
      "v_max3_f32 v5, v5, v81, v82\n"
      "v_max3_f32 v6, v6, v7, v83\n"
      "s_nop 2\n"
      "v_max3_f32 v5, v5, v84, v85\n"
      "v_max3_f32 v6, v6, v86, v87\n"
      "v_max_f32 v5, v5, v6\n"
      "v_mov_b32 v6, v5\n"
      "s_nop 1\n"
      "v_permlane16_swap_b32 v5, v6\n"
      "v_max_f32 v5, v5, v6\n"
      "v_mov_b32 v6, v5\n"
      "s_nop 1\n"
      "v_permlane32_swap_b32 v5, v6\n"
      "v_max_f32 v5, v5, v6\n"
      "v_mul_f32 v156, s64, v5\n"
      "v_fma_f32 v72, v72, s64, -v156\n"
      "v_fma_f32 v73, v73, s64, -v156\n"
      "v_fma_f32 v74, v74, s64, -v156\n"
      "v_fma_f32 v75, v75, s64, -v156\n"
      "v_fma_f32 v76, v76, s64, -v156\n"
      "v_fma_f32 v77, v77, s64, -v156\n"
      "v_fma_f32 v78, v78, s64, -v156\n"
      "v_fma_f32 v79, v79, s64, -v156\n"
      "v_fma_f32 v80, v80, s64, -v156\n"
      "v_fma_f32 v81, v81, s64, -v156\n"
      "v_fma_f32 v82, v82, s64, -v156\n"
      "v_fma_f32 v83, v83, s64, -v156\n"
      "v_fma_f32 v84, v84, s64, -v156\n"
      "v_fma_f32 v85, v85, s64, -v156\n"
      "v_fma_f32 v86, v86, s64, -v156\n"
      "v_fma_f32 v87, v87, s64, -v156\n"
      "v_exp_f32 v72, v72\n"
      "v_exp_f32 v73, v73\n"
      "v_exp_f32 v74, v74\n"
      "v_exp_f32 v75, v75\n"
      "v_exp_f32 v76, v76\n"
      "v_exp_f32 v77, v77\n"
      "v_exp_f32 v78, v78\n"
      "v_exp_f32 v79, v79\n"
      "v_exp_f32 v80, v80\n"
      "v_exp_f32 v81, v81\n"
      "v_exp_f32 v82, v82\n"
      "v_exp_f32 v83, v83\n"
      "v_exp_f32 v84, v84\n"
      "v_exp_f32 v85, v85\n"
      "v_exp_f32 v86, v86\n"
      "v_exp_f32 v87, v87\n"
      "v_pk_add_f32 v[128:129], v[72:73], v[80:81]\n"
      "v_pk_add_f32 v[130:131], v[74:75], v[82:83]\n"
      "v_pk_add_f32 v[132:133], v[76:77], v[84:85]\n"
      "v_pk_add_f32 v[134:135], v[78:79], v[86:87]\n"
      "v_pk_add_f32 v[128:129], v[128:129], v[132:133]\n"
      "v_pk_add_f32 v[130:131], v[130:131], v[134:135]\n"
      "v_pk_add_f32 v[128:129], v[128:129], v[130:131]\n"
      "v_add_f32 v157, v128, v129\n"
      "v_mov_b32 v6, v157\n"
      "s_nop 1\n"
      "v_permlane16_swap_b32 v157, v6\n"
      "v_add_f32 v157, v157, v6\n"
      "v_mov_b32 v6, v157\n"
      "s_nop 1\n"
      "v_permlane32_swap_b32 v157, v6\n"
      "v_add_f32 v157, v157, v6\n"
      "ds_write_b64 v43, v[156:157]\n"
      "s_mov_b32 exec_lo, 1\n"
      "s_mov_b32 exec_hi, 0\n"
      "ds_add_u32 v42, v55 offset:4\n"
      "s_mov_b64 exec, -1\n"
      "s_add_u32 s44, s90, 131072\n"
      "s_add_u32 m0, s41, 9216\n"
      "s_waitcnt vmcnt(3) lgkmcnt(0)\n"
      "buffer_load_dwordx4 v35, s[20:23], s44 offen nt lds\n"
      "s_add_u32 s44, s91, 131072\n"
      "s_add_u32 m0, s41, 10368\n"
      "s_waitcnt vmcnt(3) lgkmcnt(0)\n"
      "buffer_load_dwordx4 v35, s[20:23], s44 offen nt lds\n"
      "s_add_u32 s44, s92, 131072\n"
      "s_add_u32 m0, s41, 11520\n"
      "s_waitcnt vmcnt(3) lgkmcnt(0)\n"
      "buffer_load_dwordx4 v35, s[20:23], s44 offen nt lds\n"
      "s_add_u32 s44, s93, 131072\n"
      "s_add_u32 m0, s41, 12672\n"
      "s_waitcnt vmcnt(3) lgkmcnt(0)\n"
      "buffer_load_dwordx4 v35, s[20:23], s44 offen nt lds\n"
      "ds_read_b64_tr_b16 v[128:129], v50 offset:4608\n"
      "ds_read_b64_tr_b16 v[130:131], v51 offset:4608\n"
      "ds_read_b64_tr_b16 v[132:133], v52 offset:4608\n"
      "ds_read_b64_tr_b16 v[134:135], v53 offset:4608\n"
      "ds_read_b64_tr_b16 v[136:137], v50 offset:4736\n"
      "ds_read_b64_tr_b16 v[138:139], v51 offset:4736\n"
      "ds_read_b64_tr_b16 v[140:141], v52 offset:4736\n"
      "ds_read_b64_tr_b16 v[142:143], v53 offset:4736\n"
      ".Lfd_s_37:\n"
      "ds_read_b32 v5, v42 offset:4\n"
      "s_waitcnt lgkmcnt(0)\n"
      "v_cmp_ne_u32 vcc, 8, v5\n"
      "s_cbranch_vccnz .Lfd_s_37\n"
      "ds_read_b64 v[6:7], v44\n"
      "s_waitcnt lgkmcnt(0)\n"
      "v_max_f32_dpp v160, v6, v6 quad_perm:[2,3,0,1] row_mask:0xf bank_mask:0xf\n"
      "s_nop 1\n"
      "v_max_f32_dpp v160, v160, v160 row_ror:4 row_mask:0xf bank_mask:0xf\n"
      "s_nop 1\n"
      "v_max_f32_dpp v160, v160, v160 row_ror:8 row_mask:0xf bank_mask:0xf\n"
      "v_sub_f32 v6, v6, v160\n"
      "v_sub_f32 v8, v156, v160\n"
      "v_exp_f32 v6, v6\n"
      "v_exp_f32 v8, v8\n"
      "v_mul_f32 v162, v6, v7\n"
      "v_pk_mul_f32 v[72:73], v[72:73], v[8:9] op_sel_hi:[1,0]\n"
      "v_pk_mul_f32 v[74:75], v[74:75], v[8:9] op_sel_hi:[1,0]\n"
      "v_add_f32_dpp v162, v162, v162 quad_perm:[2,3,0,1] row_mask:0xf bank_mask:0xf\n"
      "v_pk_mul_f32 v[76:77], v[76:77], v[8:9] op_sel_hi:[1,0]\n"
      "v_pk_mul_f32 v[78:79], v[78:79], v[8:9] op_sel_hi:[1,0]\n"
      "v_pk_mul_f32 v[80:81], v[80:81], v[8:9] op_sel_hi:[1,0]\n"
      "v_add_f32_dpp v162, v162, v162 row_ror:4 row_mask:0xf bank_mask:0xf\n"
      "v_pk_mul_f32 v[82:83], v[82:83], v[8:9] op_sel_hi:[1,0]\n"
      "v_pk_mul_f32 v[84:85], v[84:85], v[8:9] op_sel_hi:[1,0]\n"
      "v_pk_mul_f32 v[86:87], v[86:87], v[8:9] op_sel_hi:[1,0]\n"
      "v_add_f32_dpp v162, v162, v162 row_ror:8 row_mask:0xf bank_mask:0xf\n"
      "v_cvt_pk_bf16_f32 v120, v72, v73\n"
      "v_cvt_pk_bf16_f32 v121, v74, v75\n"
      "v_cvt_pk_bf16_f32 v122, v76, v77\n"
      "v_cvt_pk_bf16_f32 v123, v78, v79\n"
      "v_cvt_pk_bf16_f32 v124, v80, v81\n"
      "v_cvt_pk_bf16_f32 v125, v82, v83\n"
      "v_cvt_pk_bf16_f32 v126, v84, v85\n"
      "v_cvt_pk_bf16_f32 v127, v86, v87\n"
      "v_mov_b32 v161, s43\n"
      "v_mov_b32 v163, s43\n"
      "buffer_store_dwordx4 v[160:163], v48, s[12:15], 0 offen sc1\n"
      "v_mfma_f32_16x16x16_bf16 v[88:91], v[120:121], v[128:129], 0\n"
      "v_mfma_f32_16x16x16_bf16 v[92:95], v[120:121], v[130:131], 0\n"
      "v_mfma_f32_16x16x16_bf16 v[96:99], v[120:121], v[132:133], 0\n"
      "v_mfma_f32_16x16x16_bf16 v[100:103], v[120:121], v[134:135], 0\n"
      "v_mfma_f32_16x16x16_bf16 v[104:107], v[120:121], v[136:137], 0\n"
      "v_mfma_f32_16x16x16_bf16 v[108:111], v[120:121], v[138:139], 0\n"
      "v_mfma_f32_16x16x16_bf16 v[112:115], v[120:121], v[140:141], 0\n"
      "v_mfma_f32_16x16x16_bf16 v[116:119], v[120:121], v[142:143], 0\n"
      "s_add_u32 s44, s90, 262144\n"
      "s_add_u32 m0, s41, 0\n"
      "s_waitcnt vmcnt(4) lgkmcnt(0)\n"
      "buffer_load_dwordx4 v35, s[20:23], s44 offen nt lds\n"
      "s_add_u32 s44, s91, 262144\n"
      "s_add_u32 m0, s41, 1152\n"
      "s_waitcnt vmcnt(4) lgkmcnt(0)\n"
      "buffer_load_dwordx4 v35, s[20:23], s44 offen nt lds\n"
      "s_add_u32 s44, s92, 262144\n"
      "s_add_u32 m0, s41, 2304\n"
      "s_waitcnt vmcnt(4) lgkmcnt(0)\n"
      "buffer_load_dwordx4 v35, s[20:23], s44 offen nt lds\n"
      "s_add_u32 s44, s93, 262144\n"
      "s_add_u32 m0, s41, 3456\n"
      "s_waitcnt vmcnt(3) lgkmcnt(0)\n"
      "buffer_load_dwordx4 v35, s[20:23], s44 offen nt lds\n"
      "ds_read_b64_tr_b16 v[128:129], v50 offset:9216\n"
      "ds_read_b64_tr_b16 v[130:131], v51 offset:9216\n"
      "ds_read_b64_tr_b16 v[132:133], v52 offset:9216\n"
      "ds_read_b64_tr_b16 v[134:135], v53 offset:9216\n"
      "ds_read_b64_tr_b16 v[136:137], v50 offset:9344\n"
      "ds_read_b64_tr_b16 v[138:139], v51 offset:9344\n"
      "ds_read_b64_tr_b16 v[140:141], v52 offset:9344\n"
      "ds_read_b64_tr_b16 v[142:143], v53 offset:9344\n"
      "s_waitcnt lgkmcnt(7)\n"
      "v_mfma_f32_16x16x16_bf16 v[88:91], v[122:123], v[128:129], v[88:91]\n"
      "s_waitcnt lgkmcnt(6)\n"
      "v_mfma_f32_16x16x16_bf16 v[92:95], v[122:123], v[130:131], v[92:95]\n"
      "s_waitcnt lgkmcnt(5)\n"
      "v_mfma_f32_16x16x16_bf16 v[96:99], v[122:123], v[132:133], v[96:99]\n"
      "s_waitcnt lgkmcnt(4)\n"
      "v_mfma_f32_16x16x16_bf16 v[100:103], v[122:123], v[134:135], v[100:103]\n"
      "s_waitcnt lgkmcnt(3)\n"
      "v_mfma_f32_16x16x16_bf16 v[104:107], v[122:123], v[136:137], v[104:107]\n"
      "s_waitcnt lgkmcnt(2)\n"
      "v_mfma_f32_16x16x16_bf16 v[108:111], v[122:123], v[138:139], v[108:111]\n"
      "s_waitcnt lgkmcnt(1)\n"
      "v_mfma_f32_16x16x16_bf16 v[112:115], v[122:123], v[140:141], v[112:115]\n"
      "s_waitcnt lgkmcnt(0)\n"
      "v_mfma_f32_16x16x16_bf16 v[116:119], v[122:123], v[142:143], v[116:119]\n"
      "s_add_u32 s44, s90, 393216\n"
      "s_add_u32 m0, s41, 4608\n"
      "s_waitcnt vmcnt(3) lgkmcnt(0)\n"
      "buffer_load_dwordx4 v35, s[20:23], s44 offen nt lds\n"
      "s_add_u32 s44, s91, 393216\n"
      "s_add_u32 m0, s41, 5760\n"
      "s_waitcnt vmcnt(3) lgkmcnt(0)\n"
      "buffer_load_dwordx4 v35, s[20:23], s44 offen nt lds\n"
      "s_add_u32 s44, s92, 393216\n"
      "s_add_u32 m0, s41, 6912\n"
      "s_waitcnt vmcnt(3) lgkmcnt(0)\n"
      "buffer_load_dwordx4 v35, s[20:23], s44 offen nt lds\n"
      "s_add_u32 s44, s93, 393216\n"
      "s_add_u32 m0, s41, 8064\n"
      "s_waitcnt vmcnt(3) lgkmcnt(0)\n"
      "buffer_load_dwordx4 v35, s[20:23], s44 offen nt lds\n"
      "ds_read_b64_tr_b16 v[128:129], v50 offset:0\n"
      "ds_read_b64_tr_b16 v[130:131], v51 offset:0\n"
      "ds_read_b64_tr_b16 v[132:133], v52 offset:0\n"
      "ds_read_b64_tr_b16 v[134:135], v53 offset:0\n"
      "ds_read_b64_tr_b16 v[136:137], v50 offset:128\n"
      "ds_read_b64_tr_b16 v[138:139], v51 offset:128\n"
      "ds_read_b64_tr_b16 v[140:141], v52 offset:128\n"
      "ds_read_b64_tr_b16 v[142:143], v53 offset:128\n"
      "s_waitcnt lgkmcnt(7)\n"
      "v_mfma_f32_16x16x16_bf16 v[88:91], v[124:125], v[128:129], v[88:91]\n"
      "s_waitcnt lgkmcnt(6)\n"
      "v_mfma_f32_16x16x16_bf16 v[92:95], v[124:125], v[130:131], v[92:95]\n"
      "s_waitcnt lgkmcnt(5)\n"
      "v_mfma_f32_16x16x16_bf16 v[96:99], v[124:125], v[132:133], v[96:99]\n"
      "s_waitcnt lgkmcnt(4)\n"
      "v_mfma_f32_16x16x16_bf16 v[100:103], v[124:125], v[134:135], v[100:103]\n"
      "s_waitcnt lgkmcnt(3)\n"
      "v_mfma_f32_16x16x16_bf16 v[104:107], v[124:125], v[136:137], v[104:107]\n"
      "s_waitcnt lgkmcnt(2)\n"
      "v_mfma_f32_16x16x16_bf16 v[108:111], v[124:125], v[138:139], v[108:111]\n"
      "s_waitcnt lgkmcnt(1)\n"
      "v_mfma_f32_16x16x16_bf16 v[112:115], v[124:125], v[140:141], v[112:115]\n"
      "s_waitcnt lgkmcnt(0)\n"
      "v_mfma_f32_16x16x16_bf16 v[116:119], v[124:125], v[142:143], v[116:119]\n"
      "s_add_u32 m0, s46, 0\n"
      "s_add_u32 s55, s63, 0\n"
      "s_waitcnt vmcnt(3) lgkmcnt(0)\n"
      "buffer_load_dwordx4 v[148:151], v49, s[12:15], 0 offen sc1\n"
      "buffer_load_dwordx4 v24, s[28:31], s55 offen nt lds\n"
      "s_add_u32 m0, s46, 1024\n"
      "s_add_u32 s55, s63, 9216\n"
      "s_waitcnt vmcnt(4) lgkmcnt(0)\n"
      "buffer_load_dwordx4 v24, s[28:31], s55 offen nt lds\n"
      "s_add_u32 m0, s46, 2048\n"
      "s_add_u32 s55, s63, 18432\n"
      "s_waitcnt vmcnt(4) lgkmcnt(0)\n"
      "buffer_load_dwordx4 v24, s[28:31], s55 offen nt lds\n"
      "s_add_u32 m0, s46, 3072\n"
      "s_add_u32 s55, s63, 27648\n"
      "s_waitcnt vmcnt(4) lgkmcnt(0)\n"
      "buffer_load_dwordx4 v24, s[28:31], s55 offen nt lds\n"
      "ds_read_b64_tr_b16 v[128:129], v50 offset:4608\n"
      "ds_read_b64_tr_b16 v[130:131], v51 offset:4608\n"
      "ds_read_b64_tr_b16 v[132:133], v52 offset:4608\n"
      "ds_read_b64_tr_b16 v[134:135], v53 offset:4608\n"
      "ds_read_b64_tr_b16 v[136:137], v50 offset:4736\n"
      "ds_read_b64_tr_b16 v[138:139], v51 offset:4736\n"
      "ds_read_b64_tr_b16 v[140:141], v52 offset:4736\n"
      "ds_read_b64_tr_b16 v[142:143], v53 offset:4736\n"
      "s_waitcnt lgkmcnt(7)\n"
      "v_mfma_f32_16x16x16_bf16 v[88:91], v[126:127], v[128:129], v[88:91]\n"
      "s_waitcnt lgkmcnt(6)\n"
      "v_mfma_f32_16x16x16_bf16 v[92:95], v[126:127], v[130:131], v[92:95]\n"
      "s_waitcnt lgkmcnt(5)\n"
      "v_mfma_f32_16x16x16_bf16 v[96:99], v[126:127], v[132:133], v[96:99]\n"
      "s_waitcnt lgkmcnt(4)\n"
      "v_mfma_f32_16x16x16_bf16 v[100:103], v[126:127], v[134:135], v[100:103]\n"
      "s_waitcnt lgkmcnt(3)\n"
      "v_mfma_f32_16x16x16_bf16 v[104:107], v[126:127], v[136:137], v[104:107]\n"
      "s_waitcnt lgkmcnt(2)\n"
      "v_mfma_f32_16x16x16_bf16 v[108:111], v[126:127], v[138:139], v[108:111]\n"
      "s_waitcnt lgkmcnt(1)\n"
      "v_mfma_f32_16x16x16_bf16 v[112:115], v[126:127], v[140:141], v[112:115]\n"
      "s_waitcnt lgkmcnt(0)\n"
      "v_mfma_f32_16x16x16_bf16 v[116:119], v[126:127], v[142:143], v[116:119]\n"
      "ds_write_b64 v45, v[88:89] offset:0\n"
      "ds_write_b64 v45, v[92:93] offset:128\n"
      "ds_write_b64 v45, v[96:97] offset:256\n"
      "ds_write_b64 v45, v[100:101] offset:384\n"
      "ds_write_b64 v45, v[104:105] offset:512\n"
      "ds_write_b64 v45, v[108:109] offset:640\n"
      "ds_write_b64 v45, v[112:113] offset:768\n"
      "s_nop 0\n"
      "ds_write_b64 v45, v[116:117] offset:896\n"
      "s_waitcnt lgkmcnt(0)\n"
      "s_barrier\n"
      "s_cmp_ge_u32 s36, 4\n"
      "s_cbranch_scc1 .Lfd_Anm\n"
      "ds_read2st64_b32 v[128:129], v46 offset0:0 offset1:4\n"
      "ds_read2st64_b32 v[130:131], v46 offset0:8 offset1:12\n"
      "ds_read2st64_b32 v[132:133], v46 offset0:16 offset1:20\n"
      "ds_read2st64_b32 v[134:135], v46 offset0:24 offset1:28\n"
      "s_waitcnt lgkmcnt(2)\n"
      "v_pk_add_f32 v[128:129], v[128:129], v[130:131]\n"
      "s_waitcnt lgkmcnt(0)\n"
      "v_pk_add_f32 v[132:133], v[132:133], v[134:135]\n"
      "v_pk_add_f32 v[128:129], v[128:129], v[132:133]\n"
      "v_add_f32 v128, v128, v129\n"
      "s_nop 1\n"
      "v_mov_b32_dpp v5, v128 quad_perm:[1,0,3,2] row_mask:0xf bank_mask:0xf\n"
      "v_cvt_pk_bf16_f32 v32, v128, v5\n"
      "s_mov_b32 exec_lo, 0x55555555\n"
      "s_mov_b32 exec_hi, 0x55555555\n"
      "buffer_store_dwordx2 v[32:33], v47, s[12:15], 0 offen sc1\n"
      "s_mov_b64 exec, -1\n"
      "s_branch .Lfd_tailw\n"
      ".Lfd_r_31:\n"
      "s_add_u32 s61, s61, 1\n"
      "s_cmp_lt_u32 s61, 4096\n"
      "s_cbranch_scc1 .Lfd_p_30\n"
      "s_branch .Lfd_d_32\n"
      ".Lfd_r_34:\n"
      "s_add_u32 s47, s47, 1\n"
      "s_cmp_lt_u32 s47, 0x10000\n"
      "s_cbranch_scc0 .Lfd_d_35\n"
      "buffer_load_dwordx4 v[144:147], v37, s[12:15], 0 offen sc1\n"
      "s_waitcnt vmcnt(0)\n"
      "v_bitop3_b32 v5, v145, v147, s43 bitop3:0x7e\n"
      "v_cmp_ne_u32 vcc, 0, v5\n"
      "s_cbranch_vccnz .Lfd_r_34\n"
      "s_branch .Lfd_d_35\n"
      ".Lfd_Anm:\n"
      "s_cmp_lg_u32 s36, 5\n"
      "s_cbranch_scc1 .Lfd_Anh\n"
      "s_mov_b32 exec_lo, 0xff\n"
      "s_mov_b32 exec_hi, 0\n"
      "buffer_store_dword v10, v164, s[12:15], 0 offen sc1\n"
      "s_mov_b64 exec, -1\n"
      ".Lfd_Anh:\n"
      "s_cmp_lg_u32 s36, 7\n"
      "s_cbranch_scc1 .Lfd_w9_38\n"
      "s_add_u32 m0, s46, 4096\n"
      "s_add_u32 s55, s63, 128\n"
      "buffer_load_dwordx4 v24, s[28:31], s55 offen nt lds\n"
      "s_add_u32 m0, s46, 5120\n"
      "s_add_u32 s55, s63, 9344\n"
      "buffer_load_dwordx4 v24, s[28:31], s55 offen nt lds\n"
      "s_add_u32 m0, s46, 6144\n"
      "s_add_u32 s55, s63, 18560\n"
      "buffer_load_dwordx4 v24, s[28:31], s55 offen nt lds\n"
      "s_add_u32 m0, s46, 7168\n"
      "s_add_u32 s55, s63, 27776\n"
      "buffer_load_dwordx4 v24, s[28:31], s55 offen nt lds\n"
      ".Lfd_w9_38:\n"
      "s_cmp_lg_u32 s36, 4\n"
      "s_cbranch_scc1 .Lfd_tailw\n"
      "s_waitcnt vmcnt(4)\n"
      ".Lfd_Aam:\n"
      "v_bitop3_b32 v5, v149, v151, s43 bitop3:0x7e\n"
      "v_cmp_ne_u32 vcc, 0, v5\n"
      "s_cbranch_vccnz .Lfd_r_39\n"
      ".Lfd_d_40:\n"
      "v_max_f32_dpp v149, v148, v148 row_ror:8 row_mask:0xf bank_mask:0xf\n"
      "v_mov_b32 v5, v149\n"
      "s_nop 1\n"
      "v_permlane16_swap_b32 v149, v5\n"
      "v_max_f32 v149, v149, v5\n"
      "v_sub_f32 v148, v148, v149\n"
      "v_exp_f32 v148, v148\n"
      "s_nop 0\n"
      "v_mul_f32 v150, v148, v150\n"
      "s_nop 1\n"
      "v_add_f32_dpp v150, v150, v150 row_ror:8 row_mask:0xf bank_mask:0xf\n"
      "v_mov_b32 v5, v150\n"
      "s_nop 1\n"
      "v_permlane16_swap_b32 v150, v5\n"
      "v_add_f32 v150, v150, v5\n"
      "v_rcp_f32 v150, v150\n"
      "s_nop 0\n"
      "v_mul_f32 v22, v148, v150\n"
      "s_lshl_b32 s58, s38, 6\n"
      "s_add_u32 s52, 0xf2000, s58\n"
      "s_mov_b32 s61, 0\n"
      ".Lfd_p_41:\n"
      "s_buffer_load_dwordx8 s[68:75], s[12:15], s52 glc\n"
      "s_waitcnt lgkmcnt(0)\n"
      "s_cmp_eq_u64 s[68:69], s[42:43]\n"
      "s_cbranch_scc0 .Lfd_r_42\n"
      "s_cmp_eq_u64 s[70:71], s[42:43]\n"
      "s_cbranch_scc0 .Lfd_r_42\n"
      "s_cmp_eq_u64 s[72:73], s[42:43]\n"
      "s_cbranch_scc0 .Lfd_r_42\n"
      "s_cmp_eq_u64 s[74:75], s[42:43]\n"
      "s_cbranch_scc0 .Lfd_r_42\n"
      ".Lfd_d_43:\n"
      ".Lfd_tailc0:\n"
      "buffer_load_dwordx4 v[72:75], v14, s[12:15], 0 offen sc1\n"
      "s_waitcnt vmcnt(1)\n"
      "ds_read_b64_tr_b16 v[40:41], v13 offset:0\n"
      "ds_read_b64_tr_b16 v[42:43], v13 offset:512\n"
      "ds_read_b64_tr_b16 v[44:45], v13 offset:32\n"
      "ds_read_b64_tr_b16 v[46:47], v13 offset:544\n"
      "ds_read_b64_tr_b16 v[48:49], v13 offset:64\n"
      "ds_read_b64_tr_b16 v[50:51], v13 offset:576\n"
      "ds_read_b64_tr_b16 v[52:53], v13 offset:96\n"
      "ds_read_b64_tr_b16 v[54:55], v13 offset:608\n"
      "s_waitcnt vmcnt(0)\n"
      ".Lfd_tailc:\n"
      "v_bitop3_b32 v5, v73, v75, s43 bitop3:0x7e\n"
      "v_cmp_ne_u32 vcc, 0, v5\n"
      "s_cbranch_vccnz .Lfd_r_44\n"
      ".Lfd_d_45:\n"
      "s_mov_b32 exec_lo, 0x1ff\n"
      "s_mov_b32 exec_hi, 0\n"
      "buffer_store_dword v10, v25, s[12:15], 0 offen sc1\n"
      "s_mov_b64 exec, -1\n"
      "v_and_b32 v73, 0xffff0000, v72\n"
      "v_lshlrev_b32 v72, 16, v72\n"
      "v_and_b32 v75, 0xffff0000, v74\n"
      "v_lshlrev_b32 v74, 16, v74\n"
      "v_pk_mul_f32 v[72:73], v[72:73], v[22:23] op_sel_hi:[1,0]\n"
      "v_pk_mul_f32 v[74:75], v[74:75], v[22:23] op_sel_hi:[1,0]\n"
      "s_nop 1\n"
      "v_permlane16_swap_b32 v72, v74\n"
      "v_permlane16_swap_b32 v73, v75\n"
      "v_pk_add_f32 v[72:73], v[72:73], v[74:75]\n"
      "s_nop 1\n"
      "v_add_f32_dpp v72, v72, v72 row_ror:8 row_mask:0xf bank_mask:0xf\n"
      "v_add_f32_dpp v73, v73, v73 row_ror:8 row_mask:0xf bank_mask:0xf\n"
      "v_cvt_pk_bf16_f32 v72, v72, v73\n"
      "ds_write_b32 v15, v72\n"
      "s_branch .Lfd_tailm\n"
      ".Lfd_r_39:\n"
      "s_add_u32 s47, s47, 1\n"
      "s_cmp_lt_u32 s47, 0x10000\n"
      "s_cbranch_scc0 .Lfd_d_40\n"
      "buffer_load_dwordx4 v[148:151], v49, s[12:15], 0 offen sc1\n"
      "s_waitcnt vmcnt(0)\n"
      "s_branch .Lfd_Aam\n"
      ".Lfd_r_42:\n"
      "s_add_u32 s61, s61, 1\n"
      "s_cmp_lt_u32 s61, 4096\n"
      "s_cbranch_scc1 .Lfd_p_41\n"
      "s_branch .Lfd_d_43\n"
      ".Lfd_r_44:\n"
      "s_add_u32 s47, s47, 1\n"
      "s_cmp_lt_u32 s47, 0x10000\n"
      "s_cbranch_scc0 .Lfd_d_45\n"
      "s_sleep 1\n"
      "buffer_load_dwordx4 v[72:75], v14, s[12:15], 0 offen sc1\n"
      "s_waitcnt vmcnt(0)\n"
      "s_branch .Lfd_tailc\n"
      ".Lfd_tailw:\n"
      "s_waitcnt vmcnt(0)\n"
      "ds_read_b64_tr_b16 v[40:41], v13 offset:0\n"
      "ds_read_b64_tr_b16 v[42:43], v13 offset:512\n"
      "ds_read_b64_tr_b16 v[44:45], v13 offset:32\n"
      "ds_read_b64_tr_b16 v[46:47], v13 offset:544\n"
      "ds_read_b64_tr_b16 v[48:49], v13 offset:64\n"
      "ds_read_b64_tr_b16 v[50:51], v13 offset:576\n"
      "ds_read_b64_tr_b16 v[52:53], v13 offset:96\n"
      "ds_read_b64_tr_b16 v[54:55], v13 offset:608\n"
      "s_cmp_lg_u32 s36, 7\n"
      "s_cbranch_scc1 .Lfd_tailm\n"
      "ds_read_b64_tr_b16 v[56:57], v13 offset:4096\n"
      "ds_read_b64_tr_b16 v[58:59], v13 offset:4608\n"
      "ds_read_b64_tr_b16 v[60:61], v13 offset:4128\n"
      "ds_read_b64_tr_b16 v[62:63], v13 offset:4640\n"
      "ds_read_b64_tr_b16 v[64:65], v13 offset:4160\n"
      "ds_read_b64_tr_b16 v[66:67], v13 offset:4672\n"
      "ds_read_b64_tr_b16 v[68:69], v13 offset:4192\n"
      "ds_read_b64_tr_b16 v[70:71], v13 offset:4704\n"
      ".Lfd_tailm:\n"
      "s_waitcnt lgkmcnt(0)\n"
      "s_barrier\n"
      "ds_read_b128 v[72:75], v16\n"
      "s_waitcnt lgkmcnt(0)\n"
      "v_mfma_f32_16x16x32_bf16 v[80:83], v[72:75], v[40:43], 0\n"
      "v_mfma_f32_16x16x32_bf16 v[84:87], v[72:75], v[44:47], 0\n"
      "v_mfma_f32_16x16x32_bf16 v[88:91], v[72:75], v[48:51], 0\n"
      "v_mfma_f32_16x16x32_bf16 v[92:95], v[72:75], v[52:55], 0\n"
      "s_nop 4\n"
      "v_cvt_pk_bf16_f32 v80, v80, v81\n"
      "v_cvt_pk_bf16_f32 v84, v84, v85\n"
      "v_cvt_pk_bf16_f32 v88, v88, v89\n"
      "v_cvt_pk_bf16_f32 v92, v92, v93\n"
      "v_cndmask_b32 v80, v80, v84, s[48:49]\n"
      "v_cndmask_b32 v88, v88, v92, s[48:49]\n"
      "v_cndmask_b32 v32, v80, v88, s[50:51]\n"
      "buffer_store_dwordx2 v[32:33], v17, s[12:15], 0 offen sc1\n"
      "s_cmp_lg_u32 s36, 7\n"
      "s_cbranch_scc1 .Lfd_t9\n"
      "s_mov_b32 s52, 66048\n"
      "v_mfma_f32_16x16x32_bf16 v[96:99], v[72:75], v[56:59], 0\n"
      "v_mfma_f32_16x16x32_bf16 v[100:103], v[72:75], v[60:63], 0\n"
      "v_mfma_f32_16x16x32_bf16 v[104:107], v[72:75], v[64:67], 0\n"
      "v_mfma_f32_16x16x32_bf16 v[108:111], v[72:75], v[68:71], 0\n"
      "s_nop 4\n"
      "v_cvt_pk_bf16_f32 v96, v96, v97\n"
      "v_cvt_pk_bf16_f32 v100, v100, v101\n"
      "v_cvt_pk_bf16_f32 v104, v104, v105\n"
      "v_cvt_pk_bf16_f32 v108, v108, v109\n"
      "v_cndmask_b32 v96, v96, v100, s[48:49]\n"
      "v_cndmask_b32 v104, v104, v108, s[48:49]\n"
      "v_cndmask_b32 v32, v96, v104, s[50:51]\n"
      "buffer_store_dwordx2 v[32:33], v17, s[12:15], s52 offen sc1\n"
      ".Lfd_t9:\n"
      "s_cmp_ge_u32 s38, 36\n"
      "s_cbranch_scc1 .Lfd_nofin\n"
      "s_lshl_b32 s58, s37, 17\n"
      "s_add_u32 s58, 0xb1000, s58\n"
      "s_lshl_b32 s57, s36, 14\n"
      "s_add_u32 s57, s58, s57\n"
      "s_lshr_b32 s58, s38, 2\n"
      "s_lshl_b32 s58, s58, 8\n"
      "s_add_u32 s52, s57, s58\n"
      "s_mov_b32 s61, 0\n"
      ".Lfd_p_46:\n"
      "s_buffer_load_dwordx4 s[68:71], s[12:15], s52 glc\n"
      "s_buffer_load_dwordx4 s[72:75], s[12:15], s52 offset:64 glc\n"
      "s_buffer_load_dwordx4 s[76:79], s[12:15], s52 offset:128 glc\n"
      "s_buffer_load_dwordx4 s[80:83], s[12:15], s52 offset:192 glc\n"
      "s_waitcnt lgkmcnt(0)\n"
      "s_cmp_eq_u64 s[68:69], s[42:43]\n"
      "s_cbranch_scc0 .Lfd_r_47\n"
      "s_cmp_eq_u64 s[70:71], s[42:43]\n"
      "s_cbranch_scc0 .Lfd_r_47\n"
      "s_cmp_eq_u64 s[72:73], s[42:43]\n"
      "s_cbranch_scc0 .Lfd_r_47\n"
      "s_cmp_eq_u64 s[74:75], s[42:43]\n"
      "s_cbranch_scc0 .Lfd_r_47\n"
      "s_cmp_eq_u64 s[76:77], s[42:43]\n"
      "s_cbranch_scc0 .Lfd_r_47\n"
      "s_cmp_eq_u64 s[78:79], s[42:43]\n"
      "s_cbranch_scc0 .Lfd_r_47\n"
      "s_cmp_eq_u64 s[80:81], s[42:43]\n"
      "s_cbranch_scc0 .Lfd_r_47\n"
      "s_cmp_eq_u64 s[82:83], s[42:43]\n"
      "s_cbranch_scc0 .Lfd_r_47\n"
      ".Lfd_d_48:\n"
      ".Lfd_fin:\n"
      "buffer_load_dwordx4 v[40:43], v18, s[12:15], 0 offen sc1\n"
      "buffer_load_dwordx4 v[44:47], v18, s[12:15], 0 offen offset:1024 sc1\n"
      "s_waitcnt vmcnt(0)\n"
      "v_bitop3_b32 v5, v41, v43, s43 bitop3:0x7e\n"
      "v_bitop3_b32 v7, v45, v47, s43 bitop3:0x7e\n"
      "v_or_b32 v5, v5, v7\n"
      "v_cmp_ne_u32 vcc, 0, v5\n"
      "s_cbranch_vccnz .Lfd_r_49\n"
      ".Lfd_d_50:\n"
      "v_lshlrev_b32 v48, 16, v40\n"
      "v_lshlrev_b32 v49, 16, v42\n"
      "v_and_b32 v50, 0xffff0000, v40\n"
      "v_and_b32 v51, 0xffff0000, v42\n"
      "v_lshlrev_b32 v52, 16, v44\n"
      "v_lshlrev_b32 v53, 16, v46\n"
      "v_and_b32 v54, 0xffff0000, v44\n"
      "v_and_b32 v55, 0xffff0000, v46\n"
      "v_pk_add_f32 v[48:49], v[48:49], v[52:53]\n"
      "v_pk_add_f32 v[50:51], v[50:51], v[54:55]\n"
      "s_nop 1\n"
      "v_permlane32_swap_b32 v48, v50\n"
      "v_permlane32_swap_b32 v49, v51\n"
      "v_pk_add_f32 v[48:49], v[48:49], v[50:51]\n"
      "s_nop 1\n"
      "v_permlane16_swap_b32 v48, v49\n"
      "v_add_f32 v48, v48, v49\n"
      "s_nop 1\n"
      "v_add_f32_dpp v48, v48, v48 row_ror:8 row_mask:0xf bank_mask:0xf\n"
      "ds_write_b32 v19, v48\n"
      "s_waitcnt lgkmcnt(0)\n"
      "s_barrier\n"
      "s_cmp_lg_u32 s36, 0\n"
      "s_cbranch_scc1 .Lfd_done\n"
      "ds_read2_b32 v[40:41], v20 offset0:0 offset1:32\n"
      "ds_read2_b32 v[42:43], v20 offset0:64 offset1:96\n"
      "ds_read2_b32 v[44:45], v20 offset0:128 offset1:160\n"
      "ds_read2_b32 v[46:47], v20 offset0:192 offset1:224\n"
      "s_waitcnt lgkmcnt(2)\n"
      "v_pk_add_f32 v[40:41], v[40:41], v[42:43]\n"
      "s_waitcnt lgkmcnt(0)\n"
      "v_pk_add_f32 v[44:45], v[44:45], v[46:47]\n"
      "v_pk_add_f32 v[40:41], v[40:41], v[44:45]\n"
      "v_add_f32 v40, v40, v41\n"
      "v_cvt_pk_bf16_f32 v40, v40, v40\n"
      "s_mov_b32 exec_hi, 0\n"
      "buffer_store_short v40, v21, s[32:35], 0 offen sc1\n"
      "s_mov_b32 exec_hi, -1\n"
      "s_branch .Lfd_done\n"
      ".Lfd_r_47:\n"
      "s_add_u32 s61, s61, 1\n"
      "s_cmp_lt_u32 s61, 4096\n"
      "s_cbranch_scc1 .Lfd_p_46\n"
      "s_branch .Lfd_d_48\n"
      ".Lfd_r_49:\n"
      "s_add_u32 s47, s47, 1\n"
      "s_cmp_lt_u32 s47, 0x10000\n"
      "s_cbranch_scc1 .Lfd_fin\n"
      "s_branch .Lfd_d_50\n"
      ".Lfd_nofin:\n"
      "s_cmp_lg_u32 s2, 255\n"
      "s_cbranch_scc1 .Lfd_done\n"
      "s_cmp_lg_u32 s36, 0\n"
      "s_cbranch_scc1 .Lfd_done\n"
      "v_lshlrev_b32 v5, 10, v0\n"
      "v_add_u32 v56, 0x1000, v5\n"
      "s_mov_b32 s52, 65536\n"
      "s_mov_b32 s53, 131072\n"
      "s_mov_b32 s54, 196608\n"
      ".Lfd_ep:\n"
      "buffer_load_dwordx4 v[40:43], v56, s[12:15], 0 offen sc1\n"
      "buffer_load_dwordx4 v[44:47], v56, s[12:15], s52 offen sc1\n"
      "buffer_load_dwordx4 v[48:51], v56, s[12:15], s53 offen sc1\n"
      "buffer_load_dwordx4 v[52:55], v56, s[12:15], s54 offen sc1\n"
      "s_waitcnt vmcnt(0)\n"
      "v_bitop3_b32 v5, v41, v43, s43 bitop3:0x7e\n"
      "v_bitop3_b32 v7, v45, v47, s43 bitop3:0x7e\n"
      "v_or_b32 v5, v5, v7\n"
      "v_bitop3_b32 v6, v49, v51, s43 bitop3:0x7e\n"
      "v_bitop3_b32 v7, v53, v55, s43 bitop3:0x7e\n"
      "v_or3_b32 v5, v5, v6, v7\n"
      "v_cmp_ne_u32 vcc, 0, v5\n"
      "s_cbranch_vccnz .Lfd_r_51\n"
      ".Lfd_d_52:\n"
      "s_mov_b32 exec_lo, 1\n"
      "s_mov_b32 exec_hi, 0\n"
      "buffer_store_dword v10, off, s[12:15], 0 sc1\n"
      "s_mov_b64 exec, -1\n"
      "s_branch .Lfd_done\n"
      ".Lfd_r_51:\n"
      "s_sleep 2\n"
      "s_add_u32 s47, s47, 1\n"
      "s_cmp_lt_u32 s47, 0x10000\n"
      "s_cbranch_scc1 .Lfd_ep\n"
      "s_branch .Lfd_d_52\n"
      ".Lfd_done:\n"
      ".Lfd_end:\n"
      : "+{v0}"(tid), "+{s2}"(wg), "+{s[0:1]}"(ka), "+{s3}"(lds)
      :
      : "v1", "v2", "v3", "v4", "v5", "v6", "v7", "v8", "v9", "v10", "v11", "v12", "v13", "v14", "v15", "v16", "v17", "v18", "v19", "v20", "v21", "v22", "v23", "v24", "v25", "v26", "v27", "v28", "v29", "v30", "v31", "v32", "v33", "v34", "v35", "v36", "v37", "v38", "v39", "v40", "v41", "v42", "v43", "v44", "v45", "v46", "v47", "v48", "v49", "v50", "v51", "v52", "v53", "v54", "v55", "v56", "v57", "v58", "v59", "v60", "v61", "v62", "v63", "v64", "v65", "v66", "v67", "v68", "v69", "v70", "v71", "v72", "v73", "v74", "v75", "v76", "v77", "v78", "v79", "v80", "v81", "v82", "v83", "v84", "v85", "v86", "v87", "v88", "v89", "v90", "v91", "v92", "v93", "v94", "v95", "v96", "v97", "v98", "v99", "v100", "v101", "v102", "v103", "v104", "v105", "v106", "v107", "v108", "v109", "v110", "v111", "v112", "v113", "v114", "v115", "v116", "v117", "v118", "v119", "v120", "v121", "v122", "v123", "v124", "v125", "v126", "v127", "v128", "v129", "v130", "v131", "v132", "v133", "v134", "v135", "v136", "v137", "v138", "v139", "v140", "v141", "v142", "v143", "v144", "v145", "v146", "v147", "v148", "v149", "v150", "v151", "v152", "v153", "v154", "v155", "v156", "v157", "v158", "v159", "v160", "v161", "v162", "v163", "v164", "v165", "v166", "v167", "v168", "v169", "v170", "v171", "v172", "v173", "v174", "v175", "v176", "v177", "v178", "v179", "v180", "v181", "v182", "v183", "v184", "v185", "v186", "v187", "v188", "v189", "v190", "v191", "v192", "v193", "v194", "v195", "v196", "v197", "v198", "v199", "v200", "v201", "v202", "v203", "v204", "v205", "v206", "v207", "v208", "v209", "v210", "v211", "v212", "v213", "v214", "v215", "v216", "v217", "v218", "v219", "v220", "v221", "v222", "v223", "v224", "v225", "v226", "v227", "v228", "v229", "v230", "v231", "v232", "v233", "v234", "v235", "v236", "v237", "v238", "v239", "v240", "v241", "v242", "v243", "v244", "v245", "v246", "v247", "v248", "v249", "v250", "v251", "v252", "v253", "v254", "v255", "s4", "s5", "s6", "s7", "s8", "s9", "s10", "s11", "s12", "s13", "s14", "s15", "s16", "s17", "s18", "s19", "s20", "s21", "s22", "s23", "s24", "s25", "s26", "s27", "s28", "s29", "s30", "s31", "s33", "s34", "s35", "s36", "s37", "s38", "s39", "s40", "s41", "s42", "s43", "s44", "s45", "s46", "s47", "s48", "s49", "s50", "s51", "s52", "s53", "s54", "s55", "s56", "s57", "s58", "s59", "s60", "s61", "s62", "s63", "s64", "s65", "s66", "s67", "s68", "s69", "s70", "s71", "s72", "s73", "s74", "s75", "s76", "s77", "s78", "s79", "s80", "s81", "s82", "s83", "s84", "s85", "s86", "s87", "s88", "s89", "s90", "s91", "s92", "s93", "s94", "s95", "s96", "s97", "s98", "s99", "vcc", "scc", "memory");
#endif
}

extern "C" void kernel_launch(void* const* d_in, const int* in_sizes, int n_in, void* d_out, int out_size, void* d_ws,
                              size_t ws_size, hipStream_t stream) {
  static const int idx[2][8] = {{3, 0, -1, 1, 2, 6, 4, -2}, {7, 5, -1, 6, 9, 8, 10, -2}};
  Args a;
  for (int r = 0; r < 2; ++r)
    for (int k = 0; k < 8; ++k) {
      const int i = idx[r][k];
      a.d[r][k].p = i >= 0 ? d_in[i] : (i == -1 ? d_ws : d_out);
      a.d[r][k].bytes = i >= 0 ? (uint32_t)in_sizes[i] * 2u : (i == -1 ? 4899840u : (uint32_t)out_size * 2u);
      a.d[r][k].fmt = 0x00020000u;
    }
  fused_decode<<<256, 512, 0, stream>>>(a);
}
```
